```python
import torch

def _softmax(x, axis=-1):
    return torch.softmax(x, dim=axis)
def _gelu(x):
    return torch.nn.functional.gelu(x, approximate="tanh")
def _silu(x):
    return torch.nn.functional.silu(x)
def _sigmoid(x):
    return torch.sigmoid(x)
def _layernorm(x, axis=-1, eps=1e-5):
    m = x.mean(dim=axis, keepdim=True)
    v = x.var(dim=axis, keepdim=True, unbiased=False)
    return (x - m) / torch.sqrt(v + eps)
def _scatter_add(tgt, idx, src, axis=0):
    return tgt.clone().index_add_(axis, idx, src)
def _segment_sum(src, seg, n):
    out = torch.zeros((n,) + src.shape[1:], dtype=src.dtype)
    return out.index_add_(0, seg, src)
def _pad(x, pw):
    flat = tuple(v for pair in reversed(pw) for v in pair)
    return torch.nn.functional.pad(x, flat)

kernel_name = 'standalone__gqa_softmax__near_ridge'

def setup_inputs():

    _w = (lambda a: (lambda t: t.to(torch.bfloat16) if t.is_floating_point() else t)(torch.as_tensor(a)))
    B, Hq, Hkv, Sq, Sk, D = 8, 32, 32, 288, 512, 192
    return {
        "q": _w((lambda *s: torch.randn(*s, dtype=torch.bfloat16) * 0.8)(B, Hq, Sq, D)),
        "k": _w((lambda *s: torch.randn(*s, dtype=torch.bfloat16) * 0.8)(B, Hkv, Sk, D)),
        "v": _w((lambda *s: torch.randn(*s, dtype=torch.bfloat16) * 0.05)(B, Hkv, Sk, D)),
    }

def reference(q, k, v):
    B, Hq, Sq, D = q.shape
    Hkv = k.shape[1]
    rep = Hq // Hkv
    k = (lambda x, n, axis: torch.repeat_interleave(x, n, dim=axis))(k, rep, axis=1)
    v = (lambda x, n, axis: torch.repeat_interleave(x, n, dim=axis))(v, rep, axis=1)
    scale = D ** -0.5
    scores = torch.einsum("bhqd,bhkd->bhqk", q, k) * scale
    p = _softmax(scores, axis=-1)
    return torch.einsum("bhqk,bhkd->bhqd", p, v)
```

```cpp
#include <hip/hip_runtime.h>
#include <hip/hip_bf16.h>
constexpr int SQ=288, SK=512, D=192;
__device__ __forceinline__ float bf2f(unsigned short x){ return __uint_as_float(((unsigned)x)<<16); }
__global__ __launch_bounds__(256) void naive(const unsigned short* __restrict__ q, const unsigned short* __restrict__ k, const unsigned short* __restrict__ v, __hip_bfloat16* __restrict__ o) {
  __shared__ float sq[8][D];
  __shared__ float sp[8][SK];
  __shared__ float red[8][2];
  int head = blockIdx.x / (SQ/8); int r0 = (blockIdx.x % (SQ/8))*8;
  const unsigned short* Q = q + ((long)head*SQ + r0)*D;
  const unsigned short* K = k + (long)head*SK*D;
  const unsigned short* V = v + (long)head*SK*D;
  int t = threadIdx.x;
  for (int i=t;i<8*D;i+=256) sq[i/D][i%D] = bf2f(Q[i]);
  __syncthreads();
  const float scale = 0.07216878364870322f;
  for (int j=t;j<SK;j+=256) {
    float acc[8]={0,0,0,0,0,0,0,0};
    for (int d=0;d<D;d++){ float kv = bf2f(K[(long)j*D+d]);
      #pragma unroll
      for (int r=0;r<8;r++) acc[r]+=sq[r][d]*kv; }
    #pragma unroll
    for (int r=0;r<8;r++) sp[r][j]=acc[r]*scale;
  }
  __syncthreads();
  if (t<8){ float m=-INFINITY; for(int j=0;j<SK;j++) m=fmaxf(m,sp[t][j]); float s=0; for(int j=0;j<SK;j++){ float e=__expf(sp[t][j]-m); sp[t][j]=e; s+=e;} red[t][0]=s; }
  __syncthreads();
  if (t<D){ float acc[8]={0,0,0,0,0,0,0,0};
    for (int j=0;j<SK;j++){ float vv=bf2f(V[(long)j*D+t]);
      #pragma unroll
      for (int r=0;r<8;r++) acc[r]+=sp[r][j]*vv; }
    #pragma unroll
    for (int r=0;r<8;r++) o[((long)head*SQ+r0+r)*D+t]=__float2bfloat16(acc[r]/red[r][0]);
  }
}
extern "C" void kernel_launch(void* const* d_in, const int* in_sizes, int n_in, void* d_out, int out_size, void* d_ws, size_t ws_size, hipStream_t stream) {
  naive<<<256*(SQ/8),256,0,stream>>>((const unsigned short*)d_in[0],(const unsigned short*)d_in[1],(const unsigned short*)d_in[2],(__hip_bfloat16*)d_out);
}
```

```cpp
#include <hip/hip_runtime.h>
#include <hip/hip_bf16.h>
typedef short bf16x8 __attribute__((ext_vector_type(8)));
typedef short s16x4 __attribute__((ext_vector_type(4)));
typedef float f32x16 __attribute__((ext_vector_type(16)));
typedef unsigned u32x4 __attribute__((ext_vector_type(4)));
typedef unsigned u32x2 __attribute__((ext_vector_type(2)));
typedef short v4i16_t __attribute__((ext_vector_type(4)));
typedef __attribute__((address_space(3))) const char* lds_cptr;
constexpr int SQ=288, SK=512, D=192, ROWB=384, TILEB=32*384;
#define MFMA(a,b,c) __builtin_amdgcn_mfma_f32_32x32x16_bf16(a,b,c,0,0,0)
__device__ __forceinline__ unsigned cvtpk(float lo, float hi){ unsigned r; asm("v_cvt_pk_bf16_f32 %0, %1, %2" : "=v"(r) : "v"(lo), "v"(hi)); return r; }
__device__ __forceinline__ void glds16(const void* g, unsigned lds_base){
  unsigned sv; asm volatile("s_mov_b32 %0, m0\n\ts_mov_b32 m0, %2\n\ts_nop 0\n\tglobal_load_lds_dwordx4 %1, off\n\ts_mov_b32 m0, %0" : "=&s"(sv) : "v"(g), "s"(lds_base) : "memory"); }
__device__ __forceinline__ int fsw(int key){ return (((key>>1)&1)<<2) | (((key>>2)&1)<<1) | ((key>>3)&1); }
__device__ __forceinline__ s16x4 vtr(lds_cptr p){ return __builtin_bit_cast(s16x4, __builtin_amdgcn_ds_read_tr16_b64_v4i16((__attribute__((address_space(3))) v4i16_t*)p)); }

__global__ __launch_bounds__(192, 2) void attn_v0(const unsigned short* __restrict__ q, const unsigned short* __restrict__ k, const unsigned short* __restrict__ v, unsigned short* __restrict__ o) {
  extern __shared__ __attribute__((aligned(16))) char lds[];
  const int tid = threadIdx.x, lane = tid & 63; const int wid = __builtin_amdgcn_readfirstlane(tid >> 6);
  const int xcd = blockIdx.x & 7, idx = blockIdx.x >> 3; const int head = (idx / 3) * 8 + xcd, part = idx % 3;
  const int r = lane & 31, h = lane >> 5;
  const int row0 = part * 96 + wid * 32;
  const char* Kh = (const char*)(k + (size_t)head * SK * D); const char* Vh = (const char*)(v + (size_t)head * SK * D);
  const unsigned lds0 = (unsigned)(uintptr_t)lds;
  int srcoff[4];
  for (int i = 0; i < 4; ++i) { int L = (wid * 4 + i) * 64 + lane; int key = L / 24, cp = L % 24; int c = cp ^ fsw(key); srcoff[i] = key * ROWB + c * 16; }
  auto dma = [&](int t, int buf) {
    for (int i = 0; i < 4; ++i) glds16(Kh + (size_t)t * TILEB + srcoff[i], (unsigned)__builtin_amdgcn_readfirstlane(lds0 + buf * 2 * TILEB + (wid * 4 + i) * 1024));
    for (int i = 0; i < 4; ++i) glds16(Vh + (size_t)t * TILEB + srcoff[i], (unsigned)__builtin_amdgcn_readfirstlane(lds0 + buf * 2 * TILEB + TILEB + (wid * 4 + i) * 1024));
  };
  dma(0, 0);
  bf16x8 qf[12];
  const unsigned short* Qw = q + ((size_t)head * SQ + row0 + r) * D + h * 8;
  #pragma unroll
  for (int s = 0; s < 12; ++s) qf[s] = *(const bf16x8*)(Qw + s * 16);
  f32x16 oacc[6];
  #pragma unroll
  for (int i = 0; i < 6; ++i) oacc[i] = f32x16{};
  float m = -1e30f, l = 0.f;
  const float C = 0.07216878364870322f * 1.4426950408889634f;
  int kaddr[4];
  for (int s = 0; s < 4; ++s) kaddr[s] = r * ROWB + 16 * ((2 * s + h) ^ fsw(r));
  const int g = lane >> 4, qq = (lane & 15) >> 2, p = lane & 3;
  int vaddr[2][2];
  for (int u = 0; u < 2; ++u) for (int dp = 0; dp < 2; ++dp) { int key = 8 * u + 4 * (g >> 1) + qq; int c = 4 * dp + 2 * (g & 1) + (p >> 1); vaddr[u][dp] = key * ROWB + 16 * (c ^ fsw(key)) + 8 * (p & 1); }
  for (int t = 0; t < SK / 32; ++t) {
    const int buf = t & 1;
    asm volatile("s_waitcnt vmcnt(0) lgkmcnt(0)\n\ts_barrier" ::: "memory");
    if (t + 1 < SK / 32) dma(t + 1, buf ^ 1);
    lds_cptr kb = (lds_cptr)lds + buf * 2 * TILEB; lds_cptr vb = kb + TILEB;
    f32x16 s16 = f32x16{};
    #pragma unroll
    for (int s = 0; s < 12; ++s) { bf16x8 kf = *(const __attribute__((address_space(3))) bf16x8*)(kb + kaddr[s & 3] + (s >> 2) * 128); s16 = MFMA(kf, qf[s], s16); }
    float mx = s16[0];
    #pragma unroll
    for (int i = 1; i < 16; ++i) mx = fmaxf(mx, s16[i]);
    { auto rr = __builtin_amdgcn_permlane32_swap(__float_as_uint(mx), __float_as_uint(mx), false, false); mx = fmaxf(__uint_as_float(rr[0]), __uint_as_float(rr[1])); }
    const float mn = fmaxf(m, mx); const float alpha = __builtin_amdgcn_exp2f((m - mn) * C); m = mn;
    const float nmc = -mn * C; float ps = 0.f;
    #pragma unroll
    for (int i = 0; i < 16; ++i) { s16[i] = __builtin_amdgcn_exp2f(fmaf(s16[i], C, nmc)); ps += s16[i]; }
    l = l * alpha + ps;
    if (__any(alpha < 1.f)) {
      #pragma unroll
      for (int i = 0; i < 6; ++i)
        #pragma unroll
        for (int j = 0; j < 16; ++j) oacc[i][j] *= alpha;
    }
    u32x4 pw[2];
    #pragma unroll
    for (int sp = 0; sp < 2; ++sp)
      #pragma unroll
      for (int j = 0; j < 4; ++j) pw[sp][j] = cvtpk(s16[8 * sp + 2 * j], s16[8 * sp + 2 * j + 1]);
    #pragma unroll
    for (int db = 0; db < 6; ++db)
      #pragma unroll
      for (int sp = 0; sp < 2; ++sp) {
        s16x4 lo = vtr(vb + vaddr[0][db & 1] + (db >> 1) * 128 + sp * 16 * ROWB), hi = vtr(vb + vaddr[1][db & 1] + (db >> 1) * 128 + sp * 16 * ROWB);
        bf16x8 vf = {lo[0], lo[1], lo[2], lo[3], hi[0], hi[1], hi[2], hi[3]};
        oacc[db] = MFMA(vf, __builtin_bit_cast(bf16x8, pw[sp]), oacc[db]);
      }
  }
  { auto rr = __builtin_amdgcn_permlane32_swap(__float_as_uint(l), __float_as_uint(l), false, false); l = __uint_as_float(rr[0]) + __uint_as_float(rr[1]); }
  const float il = 1.f / l;
  unsigned short* Ow = o + ((size_t)head * SQ + row0 + r) * D + 4 * h;
  #pragma unroll
  for (int db = 0; db < 6; ++db)
    #pragma unroll
    for (int g4 = 0; g4 < 4; ++g4) {
      u32x2 w = {cvtpk(oacc[db][4 * g4] * il, oacc[db][4 * g4 + 1] * il), cvtpk(oacc[db][4 * g4 + 2] * il, oacc[db][4 * g4 + 3] * il)};
      *(u32x2*)(Ow + 32 * db + 8 * g4) = w;
    }
}
extern "C" void kernel_launch(void* const* d_in, const int* in_sizes, int n_in, void* d_out, int out_size, void* d_ws, size_t ws_size, hipStream_t stream) {
  constexpr int LDSB = 4 * TILEB;
  (void)hipFuncSetAttribute((const void*)attn_v0, hipFuncAttributeMaxDynamicSharedMemorySize, LDSB);
  attn_v0<<<768, 192, LDSB, stream>>>((const unsigned short*)d_in[0], (const unsigned short*)d_in[1], (const unsigned short*)d_in[2], (unsigned short*)d_out);
}
```

```cpp
#include <hip/hip_runtime.h>
#include <hip/hip_bf16.h>
typedef short bf16x8 __attribute__((ext_vector_type(8)));
typedef short s16x4 __attribute__((ext_vector_type(4)));
typedef float f32x16 __attribute__((ext_vector_type(16)));
typedef unsigned u32x4 __attribute__((ext_vector_type(4)));
typedef unsigned u32x2 __attribute__((ext_vector_type(2)));
typedef short v4i16_t __attribute__((ext_vector_type(4)));
typedef __attribute__((address_space(3))) const char* lds_cptr;
constexpr int SQ = 288, SK = 512, D = 192, ROWB = 384, TILEB = 32 * 384;
constexpr int LDSB = 160064;
#define MFMA(a, b, c) __builtin_amdgcn_mfma_f32_32x32x16_bf16(a, b, c, 0, 0, 0)
__device__ __forceinline__ unsigned cvtpk(float lo, float hi) { unsigned r; asm("v_cvt_pk_bf16_f32 %0, %1, %2" : "=v"(r) : "v"(lo), "v"(hi)); return r; }
__device__ __forceinline__ void glds16(const void* g, unsigned lds_base) {
  unsigned sv;
  asm volatile("s_mov_b32 %0, m0\n\ts_mov_b32 m0, %2\n\ts_nop 0\n\tglobal_load_lds_dwordx4 %1, off\n\ts_mov_b32 m0, %0" : "=&s"(sv) : "v"(g), "s"(lds_base) : "memory");
}
__device__ __forceinline__ int fsw(int key) { return (((key >> 1) & 1) << 2) | (((key >> 2) & 1) << 1) | ((key >> 3) & 1); }
__device__ __forceinline__ s16x4 vtr(lds_cptr p) { return __builtin_bit_cast(s16x4, __builtin_amdgcn_ds_read_tr16_b64_v4i16((__attribute__((address_space(3))) v4i16_t*)p)); }

__device__ __forceinline__ void slow_path(const unsigned short* __restrict__ q, const unsigned short* __restrict__ k, const unsigned short* __restrict__ v,
                                          unsigned short* __restrict__ o, int head, int wid, char* lds) {
  const int lane = __builtin_amdgcn_mbcnt_hi(~0u, __builtin_amdgcn_mbcnt_lo(~0u, 0u));
  const int r = lane & 31, h = lane >> 5;
  const char* Kh = (const char*)(k + (size_t)head * SK * D);
  const char* Vh = (const char*)(v + (size_t)head * SK * D);
  const unsigned lds0 = (unsigned)(uintptr_t)lds;
  int srcoff[3];
  for (int i = 0; i < 3; ++i) { int L = (wid * 3 + i) * 64 + lane; int key = L / 24, cp = L % 24; srcoff[i] = key * ROWB + (cp ^ fsw(key)) * 16; }
  const float C = 0.07216878364870322f * 1.4426950408889634f;
  int kaddr[4];
  for (int s = 0; s < 4; ++s) kaddr[s] = r * ROWB + 16 * ((2 * s + h) ^ fsw(r));
  const int g = lane >> 4, qq = (lane & 15) >> 2, p = lane & 3;
  int vaddr[2][2];
  for (int u = 0; u < 2; ++u) for (int dp = 0; dp < 2; ++dp) { int key = 8 * u + 4 * (g >> 1) + qq; int c = 4 * dp + 2 * (g & 1) + (p >> 1); vaddr[u][dp] = key * ROWB + 16 * (c ^ fsw(key)) + 8 * (p & 1); }
  for (int round = 0; round < 3; ++round) {
    const int blk = round * 4 + wid;
    const bool act = blk < 9;
    const int row0 = act ? blk * 32 : 0;
    bf16x8 qf[12];
    const unsigned short* Qw = q + ((size_t)head * SQ + row0 + r) * D + h * 8;
#pragma unroll
    for (int s = 0; s < 12; ++s) qf[s] = *(const bf16x8*)(Qw + s * 16);
    f32x16 oacc[6];
#pragma unroll
    for (int i = 0; i < 6; ++i) oacc[i] = f32x16{};
    float m = -INFINITY, l = 0.f;
    for (int t = 0; t < SK / 32; ++t) {
      asm volatile("s_waitcnt vmcnt(0) lgkmcnt(0)\n\ts_barrier" ::: "memory");
      for (int i = 0; i < 3; ++i) glds16(Kh + (size_t)t * TILEB + srcoff[i], (unsigned)__builtin_amdgcn_readfirstlane(lds0 + (wid * 3 + i) * 1024));
      for (int i = 0; i < 3; ++i) glds16(Vh + (size_t)t * TILEB + srcoff[i], (unsigned)__builtin_amdgcn_readfirstlane(lds0 + TILEB + (wid * 3 + i) * 1024));
      asm volatile("s_waitcnt vmcnt(0) lgkmcnt(0)\n\ts_barrier" ::: "memory");
      lds_cptr kb = (lds_cptr)lds; lds_cptr vb = kb + TILEB;
      f32x16 s16 = f32x16{};
#pragma unroll
      for (int s = 0; s < 12; ++s) { bf16x8 kf = *(const __attribute__((address_space(3))) bf16x8*)(kb + kaddr[s & 3] + (s >> 2) * 128); s16 = MFMA(kf, qf[s], s16); }
      float mx = s16[0];
#pragma unroll
      for (int i = 1; i < 16; ++i) mx = fmaxf(mx, s16[i]);
      { auto rr = __builtin_amdgcn_permlane32_swap(__float_as_uint(mx), __float_as_uint(mx), false, false); mx = fmaxf(__uint_as_float(rr[0]), __uint_as_float(rr[1])); }
      const float mn = fmaxf(m, mx);
      const float ms = (mn == -INFINITY) ? 0.f : mn;
      const float alpha = __builtin_amdgcn_exp2f((m - ms) * C);
      m = mn;
      float ps = 0.f;
#pragma unroll
      for (int i = 0; i < 16; ++i) { s16[i] = __builtin_amdgcn_exp2f((s16[i] - ms) * C); ps += s16[i]; }
      l = l * alpha + ps;
#pragma unroll
      for (int i = 0; i < 6; ++i)
#pragma unroll
        for (int j = 0; j < 16; ++j) oacc[i][j] *= alpha;
      u32x4 pw[2];
#pragma unroll
      for (int sp = 0; sp < 2; ++sp)
#pragma unroll
        for (int j = 0; j < 4; ++j) pw[sp][j] = cvtpk(s16[8 * sp + 2 * j], s16[8 * sp + 2 * j + 1]);
#pragma unroll
      for (int db = 0; db < 6; ++db)
#pragma unroll
        for (int sp = 0; sp < 2; ++sp) {
          s16x4 lo = vtr(vb + vaddr[0][db & 1] + (db >> 1) * 128 + sp * 16 * ROWB), hi = vtr(vb + vaddr[1][db & 1] + (db >> 1) * 128 + sp * 16 * ROWB);
          bf16x8 vf = {lo[0], lo[1], lo[2], lo[3], hi[0], hi[1], hi[2], hi[3]};
          oacc[db] = MFMA(vf, __builtin_bit_cast(bf16x8, pw[sp]), oacc[db]);
        }
    }
    { auto rr = __builtin_amdgcn_permlane32_swap(__float_as_uint(l), __float_as_uint(l), false, false); l = __uint_as_float(rr[0]) + __uint_as_float(rr[1]); }
    const float il = 1.f / l;
    if (act) {
      unsigned short* Ow = o + ((size_t)head * SQ + row0 + r) * D + 4 * h;
#pragma unroll
      for (int db = 0; db < 6; ++db)
#pragma unroll
        for (int g4 = 0; g4 < 4; ++g4) {
          u32x2 w = {cvtpk(oacc[db][4 * g4] * il, oacc[db][4 * g4 + 1] * il), cvtpk(oacc[db][4 * g4 + 2] * il, oacc[db][4 * g4 + 3] * il)};
          *(u32x2*)(Ow + 32 * db + 8 * g4) = w;
        }
    }
  }
}

typedef unsigned u32x8 __attribute__((ext_vector_type(8)));
__global__ __launch_bounds__(256, 1) void attn_fwd(const unsigned short* __restrict__ q_, const unsigned short* __restrict__ k_, const unsigned short* __restrict__ v_,
                                                   unsigned short* __restrict__ o_
) {
  const unsigned short* q = q_; const unsigned short* k = k_; const unsigned short* v = v_; unsigned short* o = o_;
  extern __shared__ __attribute__((aligned(16))) char lds[];
  const int wid = __builtin_amdgcn_readfirstlane(threadIdx.x >> 6);
  const int head = blockIdx.x;
  const unsigned lds0 = (unsigned)(uintptr_t)lds;
  unsigned unsafe;
  asm volatile(
"s_load_dwordx4 s[40:43], %1, 0\n"
"s_load_dwordx2 s[44:45], %1, 16\n"
"s_load_dwordx2 s[38:39], %1, 24\n"
"v_mbcnt_lo_u32_b32 v160, -1, 0\n"
"v_mbcnt_hi_u32_b32 v160, -1, v160\n"
"s_getpc_b64 s[76:77]\n"
"s_mul_i32 s78, %3, 3072\n"
"v_min_u32_e32 v255, 23, v160\n"
"v_lshlrev_b32_e32 v255, 7, v255\n"
"v_add_u32_e32 v255, s78, v255\n"
"global_load_dword v200, v255, s[76:77]\n"
"s_mov_b32 s67, %3\n"
"s_mov_b32 s72, %4\n"
"s_mul_i32 s70, %2, 110592\n"
"s_mul_i32 s69, %2, 196608\n"
"s_mov_b32 s50, 110592\n"
"s_mov_b32 s51, 0x20000\n"
"v_add_u32_e32 v161, 0, v160\n"
"v_mul_u32_u24_e32 v162, 0xaab, v161\n"
"v_lshrrev_b32_e32 v162, 16, v162\n"
"v_mul_u32_u24_e32 v163, 24, v162\n"
"v_sub_u32_e32 v163, v161, v163\n"
"v_bfrev_b32_e32 v164, v162\n"
"v_bfe_u32 v164, v164, 28, 3\n"
"v_xor_b32_e32 v163, v163, v164\n"
"v_lshlrev_b32_e32 v163, 4, v163\n"
"v_mul_u32_u24_e32 v162, 0x180, v162\n"
"v_add_u32_e32 v163, v163, v162\n"
"v_subrev_u32_e32 v165, 0, v163\n"
"v_xor_b32_e32 v168, 16, v165\n"
"v_add_u32_e32 v161, 64, v160\n"
"v_mul_u32_u24_e32 v162, 0xaab, v161\n"
"v_lshrrev_b32_e32 v162, 16, v162\n"
"v_mul_u32_u24_e32 v163, 24, v162\n"
"v_sub_u32_e32 v163, v161, v163\n"
"v_bfrev_b32_e32 v164, v162\n"
"v_bfe_u32 v164, v164, 28, 3\n"
"v_xor_b32_e32 v163, v163, v164\n"
"v_lshlrev_b32_e32 v163, 4, v163\n"
"v_mul_u32_u24_e32 v162, 0x180, v162\n"
"v_add_u32_e32 v163, v163, v162\n"
"v_subrev_u32_e32 v166, 1024, v163\n"
"v_xor_b32_e32 v169, 16, v166\n"
"v_add_u32_e32 v161, 128, v160\n"
"v_mul_u32_u24_e32 v162, 0xaab, v161\n"
"v_lshrrev_b32_e32 v162, 16, v162\n"
"v_mul_u32_u24_e32 v163, 24, v162\n"
"v_sub_u32_e32 v163, v161, v163\n"
"v_bfrev_b32_e32 v164, v162\n"
"v_bfe_u32 v164, v164, 28, 3\n"
"v_xor_b32_e32 v163, v163, v164\n"
"v_lshlrev_b32_e32 v163, 4, v163\n"
"v_mul_u32_u24_e32 v162, 0x180, v162\n"
"v_add_u32_e32 v163, v163, v162\n"
"v_subrev_u32_e32 v167, 2048, v163\n"
"v_xor_b32_e32 v170, 16, v167\n"
"s_mul_i32 s76, s67, 24576\n"
"s_mul_i32 s77, s67, 6144\n"
"s_add_u32 s77, s77, s72\n"
"s_and_b32 s71, s67, 1\n"
"s_lshl_b32 s71, s71, 4\n"
"s_mul_i32 s73, s67, 3072\n"
"v_xor_b32_e32 v246, s71, v165\n"
"v_add_u32_e32 v246, s73, v246\n"
"v_xor_b32_e32 v247, s71, v166\n"
"v_add_u32_e32 v247, s73, v247\n"
"v_xor_b32_e32 v248, s71, v167\n"
"v_add_u32_e32 v248, s73, v248\n"
"s_add_u32 s58, s72, s73\n"
"s_add_u32 s59, s58, 12288\n"
"s_mov_b32 s47, 0x20000\n"
"s_waitcnt lgkmcnt(0)\n"
"s_add_u32 s48, s40, s70\n"
"s_addc_u32 s49, s41, 0\n"
"s_and_b32 s49, s49, 0xffff\n"
"s_add_u32 s40, s42, s69\n"
"s_addc_u32 s41, s43, 0\n"
"s_and_b32 s41, s41, 0xffff\n"
"s_mov_b32 s42, 196608\n"
"s_mov_b32 s43, 0x20000\n"
"s_mov_b32 s73, 0\n"
"s_add_u32 m0, s58, 0\n"
"s_nop 0\n"
"buffer_load_dwordx4 v246, s[40:43], s73 offen offset:0 nt lds\n"
"buffer_load_dwordx4 v247, s[40:43], s73 offen offset:1024 nt lds\n"
"buffer_load_dwordx4 v248, s[40:43], s73 offen offset:2048 nt lds\n"
"s_mov_b32 s73, 12288\n"
"s_add_u32 m0, s59, 0\n"
"s_nop 0\n"
"buffer_load_dwordx4 v246, s[40:43], s73 offen offset:0 nt lds\n"
"buffer_load_dwordx4 v247, s[40:43], s73 offen offset:1024 nt lds\n"
"buffer_load_dwordx4 v248, s[40:43], s73 offen offset:2048 nt lds\n"
"s_add_u32 s60, s58, 73728\n"
"s_add_u32 s61, s60, 12288\n"
"s_sub_u32 s69, s69, 49152\n"
"s_ashr_i32 s70, s69, 31\n"
"s_add_u32 s44, s44, s69\n"
"s_addc_u32 s45, s45, s70\n"
"s_and_b32 s45, s45, 0xffff\n"
"s_mov_b32 s46, 245760\n"
"s_mov_b32 s52, 0x3dd53b95\n"
"s_mov_b32 s53, 0x3dd53b95\n"
"s_mov_b32 s54, 0xffff0000\n"
"s_lshr_b32 s68, s67, 1\n"
"s_mul_i32 s62, s68, -24576\n"
"s_add_u32 s62, s62, 12288\n"
"s_sub_u32 s65, 24576, s62\n"
"s_sub_u32 s66, -49152, s62\n"
"s_mov_b32 s55, 73728\n"
"s_mov_b32 s56, 86016\n"
"s_mov_b32 s57, 0\n"
"s_mov_b32 s79, 49152\n"
"v_and_b32_e32 v171, 15, v160\n"
"v_lshrrev_b32_e32 v172, 4, v160\n"
"v_bfrev_b32_e32 v173, v171\n"
"v_bfe_u32 v173, v173, 28, 3\n"
"v_xor_b32_e32 v161, v172, v173\n"
"v_lshlrev_b32_e32 v161, 4, v161\n"
"v_mul_u32_u24_e32 v174, 0x180, v171\n"
"v_add_u32_e32 v174, v174, v161\n"
"s_mul_i32 s70, s68, 12288\n"
"s_add_u32 s70, s70, s72\n"
"v_add_u32_e32 v236, s70, v174\n"
"v_xor_b32_e32 v237, 64, v236\n"
"v_add_u32_e32 v181, s62, v236\n"
"v_add_u32_e32 v182, s62, v237\n"
"v_lshrrev_b32_e32 v161, 2, v160\n"
"v_bfrev_b32_e32 v162, v161\n"
"v_bfe_u32 v162, v162, 28, 3\n"
"v_bfe_u32 v163, v160, 1, 1\n"
"v_xor_b32_e32 v163, v163, v162\n"
"v_lshlrev_b32_e32 v163, 4, v163\n"
"v_mul_u32_u24_e32 v161, 0x180, v161\n"
"v_add_u32_e32 v161, v161, v163\n"
"v_and_b32_e32 v163, 1, v160\n"
"v_lshlrev_b32_e32 v163, 3, v163\n"
"v_add_u32_e32 v161, v161, v163\n"
"s_add_u32 s70, s70, 98304\n"
"v_add_u32_e32 v238, s70, v161\n"
"s_lshl_b32 s71, s68, 5\n"
"v_xor_b32_e32 v238, s71, v238\n"
"v_xor_b32_e32 v239, 32, v238\n"
"v_xor_b32_e32 v240, 64, v238\n"
"v_xor_b32_e32 v241, 96, v238\n"
"v_add_u32_e32 v242, s62, v238\n"
"v_add_u32_e32 v243, s62, v239\n"
"v_add_u32_e32 v244, s62, v240\n"
"v_add_u32_e32 v245, s62, v241\n"
"v_mov_b32_e32 v249, 0\n"
"v_mov_b32_e32 v250, 0\n"
"v_mov_b32_e32 v251, 0\n"
"v_mov_b32_e32 v252, 0\n"
"v_mov_b32_e32 v253, 0\n"
"v_add_u32_e32 v175, s77, v174\n"
"v_xor_b32_e32 v176, 64, v175\n"
"v_add_u32_e32 v177, 73728, v175\n"
"v_xor_b32_e32 v178, 64, v177\n"
"s_and_b32 s70, s67, 1\n"
"s_mul_i32 s70, s70, 6144\n"
"s_add_u32 s70, s70, s72\n"
"s_add_u32 s70, s70, 147456\n"
"v_add_u32_e32 v179, s70, v174\n"
"v_xor_b32_e32 v180, 64, v179\n"
"s_waitcnt vmcnt(6)\n"
"s_add_u32 s73, s76, 0\n"
"s_add_u32 m0, s77, 24576\n"
"s_nop 0\n"
"buffer_load_dwordx4 v165, s[48:51], s73 offen offset:0 nt lds\n"
"buffer_load_dwordx4 v166, s[48:51], s73 offen offset:1024 nt lds\n"
"buffer_load_dwordx4 v167, s[48:51], s73 offen offset:2048 nt lds\n"
"s_waitcnt vmcnt(6)\n"
"s_add_u32 s73, s76, 3072\n"
"s_add_u32 m0, s77, 27648\n"
"s_nop 0\n"
"buffer_load_dwordx4 v168, s[48:51], s73 offen offset:0 nt lds\n"
"buffer_load_dwordx4 v169, s[48:51], s73 offen offset:1024 nt lds\n"
"buffer_load_dwordx4 v170, s[48:51], s73 offen offset:2048 nt lds\n"
"s_mul_i32 s73, %2, 110592\n"
"s_mul_i32 s74, %3, 24576\n"
"s_add_u32 s73, s73, s74\n"
"v_mov_b32_e32 v254, s73\n"
"global_load_dword v255, v254, s[38:39]\n"
"s_waitcnt vmcnt(7)\n"
"s_barrier\n"
"ds_read_b128 a[120:123], v236 offset:0\n"
"ds_read_b128 a[124:127], v237 offset:0\n"
"ds_read_b128 a[128:131], v236 offset:128\n"
"ds_read_b128 a[132:135], v237 offset:128\n"
"ds_read_b128 a[136:139], v236 offset:256\n"
"ds_read_b128 a[140:143], v237 offset:256\n"
"ds_read_b128 a[144:147], v236 offset:6144\n"
"ds_read_b128 a[148:151], v237 offset:6144\n"
"ds_read_b128 a[152:155], v236 offset:6272\n"
"ds_read_b128 a[156:159], v237 offset:6272\n"
"ds_read_b128 a[160:163], v236 offset:6400\n"
"ds_read_b128 a[164:167], v237 offset:6400\n"
"ds_read_b128 a[168:171], v181 offset:0\n"
"ds_read_b128 a[172:175], v182 offset:0\n"
"ds_read_b128 a[176:179], v181 offset:128\n"
"s_waitcnt lgkmcnt(14)\n"
"ds_read_b128 a[180:183], v182 offset:128\n"
"s_waitcnt lgkmcnt(14)\n"
"ds_read_b128 a[184:187], v181 offset:256\n"
"s_waitcnt lgkmcnt(14)\n"
"ds_read_b128 a[188:191], v182 offset:256\n"
"s_waitcnt lgkmcnt(14)\n"
"ds_read_b128 a[192:195], v181 offset:6144\n"
"s_waitcnt lgkmcnt(14)\n"
"ds_read_b128 a[196:199], v182 offset:6144\n"
"s_waitcnt lgkmcnt(14)\n"
"ds_read_b128 a[200:203], v181 offset:6272\n"
"s_waitcnt lgkmcnt(14)\n"
"ds_read_b128 a[204:207], v182 offset:6272\n"
"s_waitcnt lgkmcnt(14)\n"
"ds_read_b128 a[208:211], v181 offset:6400\n"
"s_waitcnt lgkmcnt(14)\n"
"ds_read_b128 a[212:215], v182 offset:6400\n"
"v_add_u32_e32 v236, 24576, v236\n"
"v_add_u32_e32 v237, 24576, v237\n"
"s_waitcnt vmcnt(6)\n"
"s_add_u32 s73, s76, 6144\n"
"s_add_u32 m0, s77, 49152\n"
"s_nop 0\n"
"buffer_load_dwordx4 v165, s[48:51], s73 offen offset:0 nt lds\n"
"buffer_load_dwordx4 v166, s[48:51], s73 offen offset:1024 nt lds\n"
"buffer_load_dwordx4 v167, s[48:51], s73 offen offset:2048 nt lds\n"
"s_waitcnt vmcnt(6)\n"
"s_add_u32 s73, s76, 9216\n"
"s_add_u32 m0, s77, 52224\n"
"s_nop 0\n"
"buffer_load_dwordx4 v168, s[48:51], s73 offen offset:0 nt lds\n"
"buffer_load_dwordx4 v169, s[48:51], s73 offen offset:1024 nt lds\n"
"buffer_load_dwordx4 v170, s[48:51], s73 offen offset:2048 nt lds\n"
"s_waitcnt vmcnt(7)\n"
"s_waitcnt lgkmcnt(14)\n"
"ds_read_b128 v[0:3], v175 offset:24576\n"
"s_waitcnt lgkmcnt(14)\n"
"ds_read_b128 v[4:7], v176 offset:24576\n"
"s_waitcnt lgkmcnt(14)\n"
"ds_read_b128 v[8:11], v175 offset:24704\n"
"s_waitcnt lgkmcnt(14)\n"
"ds_read_b128 v[12:15], v176 offset:24704\n"
"s_waitcnt lgkmcnt(14)\n"
"ds_read_b128 v[16:19], v175 offset:24832\n"
"s_waitcnt lgkmcnt(14)\n"
"ds_read_b128 v[20:23], v176 offset:24832\n"
"s_waitcnt lgkmcnt(5)\n"
"v_lshlrev_b32_e32 v24, 16, v0\n"
"v_and_b32_e32 v25, s54, v0\n"
"v_lshlrev_b32_e32 v26, 16, v1\n"
"v_and_b32_e32 v27, s54, v1\n"
"v_lshlrev_b32_e32 v28, 16, v2\n"
"v_and_b32_e32 v29, s54, v2\n"
"v_lshlrev_b32_e32 v30, 16, v3\n"
"v_and_b32_e32 v31, s54, v3\n"
"v_pk_mul_f32 v[24:25], v[24:25], s[52:53]\n"
"v_pk_mul_f32 v[26:27], v[26:27], s[52:53]\n"
"v_pk_mul_f32 v[28:29], v[28:29], s[52:53]\n"
"v_pk_mul_f32 v[30:31], v[30:31], s[52:53]\n"
"v_cvt_pk_bf16_f32 v0, v24, v25\n"
"v_cvt_pk_bf16_f32 v1, v26, v27\n"
"v_cvt_pk_bf16_f32 v2, v28, v29\n"
"v_cvt_pk_bf16_f32 v3, v30, v31\n"
"v_accvgpr_write_b32 a0, v0\n"
"v_accvgpr_write_b32 a1, v1\n"
"v_accvgpr_write_b32 a2, v2\n"
"v_accvgpr_write_b32 a3, v3\n"
"s_waitcnt lgkmcnt(4)\n"
"v_lshlrev_b32_e32 v24, 16, v4\n"
"v_and_b32_e32 v25, s54, v4\n"
"v_lshlrev_b32_e32 v26, 16, v5\n"
"v_and_b32_e32 v27, s54, v5\n"
"v_mfma_f32_16x16x32_bf16 v[128:131], a[120:123], v[0:3], 0\n"
"v_lshlrev_b32_e32 v28, 16, v6\n"
"v_and_b32_e32 v29, s54, v6\n"
"v_lshlrev_b32_e32 v30, 16, v7\n"
"v_and_b32_e32 v31, s54, v7\n"
"v_mfma_f32_16x16x32_bf16 v[192:195], a[144:147], v[0:3], 0\n"
"v_pk_mul_f32 v[24:25], v[24:25], s[52:53]\n"
"v_pk_mul_f32 v[26:27], v[26:27], s[52:53]\n"
"v_pk_mul_f32 v[28:29], v[28:29], s[52:53]\n"
"v_pk_mul_f32 v[30:31], v[30:31], s[52:53]\n"
"v_mfma_f32_16x16x32_bf16 v[144:147], a[168:171], v[0:3], 0\n"
"v_cvt_pk_bf16_f32 v4, v24, v25\n"
"v_cvt_pk_bf16_f32 v5, v26, v27\n"
"v_cvt_pk_bf16_f32 v6, v28, v29\n"
"v_cvt_pk_bf16_f32 v7, v30, v31\n"
"v_mfma_f32_16x16x32_bf16 v[208:211], a[192:195], v[0:3], 0\n"
"s_waitcnt lgkmcnt(3)\n"
"v_accvgpr_write_b32 a4, v4\n"
"v_accvgpr_write_b32 a5, v5\n"
"v_accvgpr_write_b32 a6, v6\n"
"v_accvgpr_write_b32 a7, v7\n"
"v_lshlrev_b32_e32 v24, 16, v8\n"
"v_and_b32_e32 v25, s54, v8\n"
"v_lshlrev_b32_e32 v26, 16, v9\n"
"v_and_b32_e32 v27, s54, v9\n"
"v_mfma_f32_16x16x32_bf16 v[128:131], a[124:127], v[4:7], v[128:131]\n"
"v_lshlrev_b32_e32 v28, 16, v10\n"
"v_and_b32_e32 v29, s54, v10\n"
"v_lshlrev_b32_e32 v30, 16, v11\n"
"v_and_b32_e32 v31, s54, v11\n"
"v_mfma_f32_16x16x32_bf16 v[192:195], a[148:151], v[4:7], v[192:195]\n"
"v_pk_mul_f32 v[24:25], v[24:25], s[52:53]\n"
"v_pk_mul_f32 v[26:27], v[26:27], s[52:53]\n"
"v_pk_mul_f32 v[28:29], v[28:29], s[52:53]\n"
"v_pk_mul_f32 v[30:31], v[30:31], s[52:53]\n"
"v_mfma_f32_16x16x32_bf16 v[144:147], a[172:175], v[4:7], v[144:147]\n"
"v_cvt_pk_bf16_f32 v8, v24, v25\n"
"v_cvt_pk_bf16_f32 v9, v26, v27\n"
"v_cvt_pk_bf16_f32 v10, v28, v29\n"
"v_cvt_pk_bf16_f32 v11, v30, v31\n"
"v_mfma_f32_16x16x32_bf16 v[208:211], a[196:199], v[4:7], v[208:211]\n"
"s_waitcnt lgkmcnt(2)\n"
"v_accvgpr_write_b32 a8, v8\n"
"v_accvgpr_write_b32 a9, v9\n"
"v_accvgpr_write_b32 a10, v10\n"
"v_accvgpr_write_b32 a11, v11\n"
"v_lshlrev_b32_e32 v24, 16, v12\n"
"v_and_b32_e32 v25, s54, v12\n"
"v_lshlrev_b32_e32 v26, 16, v13\n"
"v_and_b32_e32 v27, s54, v13\n"
"s_waitcnt vmcnt(6)\n"
"s_mov_b32 s73, 98304\n"
"s_add_u32 m0, s58, 147456\n"
"s_nop 0\n"
"buffer_load_dwordx4 v246, s[48:51], s73 offen offset:0 nt lds\n"
"buffer_load_dwordx4 v247, s[48:51], s73 offen offset:1024 nt lds\n"
"buffer_load_dwordx4 v248, s[48:51], s73 offen offset:2048 nt lds\n"
"v_mfma_f32_16x16x32_bf16 v[128:131], a[128:131], v[8:11], v[128:131]\n"
"v_lshlrev_b32_e32 v28, 16, v14\n"
"v_and_b32_e32 v29, s54, v14\n"
"v_lshlrev_b32_e32 v30, 16, v15\n"
"v_and_b32_e32 v31, s54, v15\n"
"v_mfma_f32_16x16x32_bf16 v[192:195], a[152:155], v[8:11], v[192:195]\n"
"v_pk_mul_f32 v[24:25], v[24:25], s[52:53]\n"
"v_pk_mul_f32 v[26:27], v[26:27], s[52:53]\n"
"v_pk_mul_f32 v[28:29], v[28:29], s[52:53]\n"
"v_pk_mul_f32 v[30:31], v[30:31], s[52:53]\n"
"v_mfma_f32_16x16x32_bf16 v[144:147], a[176:179], v[8:11], v[144:147]\n"
"v_cvt_pk_bf16_f32 v12, v24, v25\n"
"v_cvt_pk_bf16_f32 v13, v26, v27\n"
"v_cvt_pk_bf16_f32 v14, v28, v29\n"
"v_cvt_pk_bf16_f32 v15, v30, v31\n"
"v_mfma_f32_16x16x32_bf16 v[208:211], a[200:203], v[8:11], v[208:211]\n"
"s_waitcnt lgkmcnt(1)\n"
"v_accvgpr_write_b32 a12, v12\n"
"v_accvgpr_write_b32 a13, v13\n"
"v_accvgpr_write_b32 a14, v14\n"
"v_accvgpr_write_b32 a15, v15\n"
"v_lshlrev_b32_e32 v24, 16, v16\n"
"v_and_b32_e32 v25, s54, v16\n"
"v_lshlrev_b32_e32 v26, 16, v17\n"
"v_and_b32_e32 v27, s54, v17\n"
"v_mfma_f32_16x16x32_bf16 v[128:131], a[132:135], v[12:15], v[128:131]\n"
"v_lshlrev_b32_e32 v28, 16, v18\n"
"v_and_b32_e32 v29, s54, v18\n"
"v_lshlrev_b32_e32 v30, 16, v19\n"
"v_and_b32_e32 v31, s54, v19\n"
"v_mfma_f32_16x16x32_bf16 v[192:195], a[156:159], v[12:15], v[192:195]\n"
"v_pk_mul_f32 v[24:25], v[24:25], s[52:53]\n"
"v_pk_mul_f32 v[26:27], v[26:27], s[52:53]\n"
"v_pk_mul_f32 v[28:29], v[28:29], s[52:53]\n"
"v_pk_mul_f32 v[30:31], v[30:31], s[52:53]\n"
"v_mfma_f32_16x16x32_bf16 v[144:147], a[180:183], v[12:15], v[144:147]\n"
"v_cvt_pk_bf16_f32 v16, v24, v25\n"
"v_cvt_pk_bf16_f32 v17, v26, v27\n"
"v_cvt_pk_bf16_f32 v18, v28, v29\n"
"v_cvt_pk_bf16_f32 v19, v30, v31\n"
"v_mfma_f32_16x16x32_bf16 v[208:211], a[204:207], v[12:15], v[208:211]\n"
"s_waitcnt lgkmcnt(0)\n"
"v_accvgpr_write_b32 a16, v16\n"
"v_accvgpr_write_b32 a17, v17\n"
"v_accvgpr_write_b32 a18, v18\n"
"v_accvgpr_write_b32 a19, v19\n"
"v_lshlrev_b32_e32 v24, 16, v20\n"
"v_and_b32_e32 v25, s54, v20\n"
"v_lshlrev_b32_e32 v26, 16, v21\n"
"v_and_b32_e32 v27, s54, v21\n"
"v_mfma_f32_16x16x32_bf16 v[128:131], a[136:139], v[16:19], v[128:131]\n"
"v_lshlrev_b32_e32 v28, 16, v22\n"
"v_and_b32_e32 v29, s54, v22\n"
"v_lshlrev_b32_e32 v30, 16, v23\n"
"v_and_b32_e32 v31, s54, v23\n"
"v_mfma_f32_16x16x32_bf16 v[192:195], a[160:163], v[16:19], v[192:195]\n"
"v_pk_mul_f32 v[24:25], v[24:25], s[52:53]\n"
"v_pk_mul_f32 v[26:27], v[26:27], s[52:53]\n"
"v_pk_mul_f32 v[28:29], v[28:29], s[52:53]\n"
"v_pk_mul_f32 v[30:31], v[30:31], s[52:53]\n"
"v_mfma_f32_16x16x32_bf16 v[144:147], a[184:187], v[16:19], v[144:147]\n"
"v_cvt_pk_bf16_f32 v20, v24, v25\n"
"v_cvt_pk_bf16_f32 v21, v26, v27\n"
"v_cvt_pk_bf16_f32 v22, v28, v29\n"
"v_cvt_pk_bf16_f32 v23, v30, v31\n"
"v_mfma_f32_16x16x32_bf16 v[208:211], a[208:211], v[16:19], v[208:211]\n"
"v_accvgpr_write_b32 a20, v20\n"
"v_accvgpr_write_b32 a21, v21\n"
"v_accvgpr_write_b32 a22, v22\n"
"v_accvgpr_write_b32 a23, v23\n"
"v_mfma_f32_16x16x32_bf16 v[128:131], a[140:143], v[20:23], v[128:131]\n"
"v_mfma_f32_16x16x32_bf16 v[192:195], a[164:167], v[20:23], v[192:195]\n"
"v_mfma_f32_16x16x32_bf16 v[144:147], a[188:191], v[20:23], v[144:147]\n"
"v_mfma_f32_16x16x32_bf16 v[208:211], a[212:215], v[20:23], v[208:211]\n"
"s_waitcnt vmcnt(6)\n"
"s_add_u32 s73, s76, 12288\n"
"s_add_u32 m0, s77, 122880\n"
"s_nop 0\n"
"buffer_load_dwordx4 v165, s[48:51], s73 offen offset:0 nt lds\n"
"buffer_load_dwordx4 v166, s[48:51], s73 offen offset:1024 nt lds\n"
"buffer_load_dwordx4 v167, s[48:51], s73 offen offset:2048 nt lds\n"
"s_waitcnt vmcnt(6)\n"
"ds_read_b128 v[48:51], v175 offset:49152\n"
"ds_read_b128 v[52:55], v176 offset:49152\n"
"ds_read_b128 v[56:59], v175 offset:49280\n"
"ds_read_b128 v[60:63], v176 offset:49280\n"
"ds_read_b128 v[64:67], v175 offset:49408\n"
"ds_read_b128 v[68:71], v176 offset:49408\n"
"v_exp_f32_e32 v128, v128\n"
"v_exp_f32_e32 v129, v129\n"
"v_exp_f32_e32 v130, v130\n"
"v_exp_f32_e32 v131, v131\n"
"v_exp_f32_e32 v192, v192\n"
"v_exp_f32_e32 v193, v193\n"
"v_exp_f32_e32 v194, v194\n"
"v_exp_f32_e32 v195, v195\n"
"v_add_f32_e32 v249, v249, v128\n"
"v_add_f32_e32 v249, v249, v129\n"
"v_add_f32_e32 v249, v249, v130\n"
"v_add_f32_e32 v249, v249, v131\n"
"v_add_f32_e32 v249, v249, v192\n"
"v_add_f32_e32 v249, v249, v193\n"
"v_add_f32_e32 v249, v249, v194\n"
"v_add_f32_e32 v249, v249, v195\n"
"v_cvt_pk_bf16_f32 v128, v128, v129\n"
"v_cvt_pk_bf16_f32 v129, v130, v131\n"
"v_cvt_pk_bf16_f32 v130, v192, v193\n"
"v_cvt_pk_bf16_f32 v131, v194, v195\n"
"v_exp_f32_e32 v144, v144\n"
"v_exp_f32_e32 v145, v145\n"
"v_exp_f32_e32 v146, v146\n"
"v_exp_f32_e32 v147, v147\n"
"v_exp_f32_e32 v208, v208\n"
"v_exp_f32_e32 v209, v209\n"
"v_exp_f32_e32 v210, v210\n"
"v_exp_f32_e32 v211, v211\n"
"v_add_f32_e32 v249, v249, v144\n"
"v_add_f32_e32 v249, v249, v145\n"
"v_add_f32_e32 v249, v249, v146\n"
"v_add_f32_e32 v249, v249, v147\n"
"v_add_f32_e32 v249, v249, v208\n"
"v_add_f32_e32 v249, v249, v209\n"
"v_add_f32_e32 v249, v249, v210\n"
"v_add_f32_e32 v249, v249, v211\n"
"v_cvt_pk_bf16_f32 v144, v144, v145\n"
"v_cvt_pk_bf16_f32 v145, v146, v147\n"
"v_cvt_pk_bf16_f32 v146, v208, v209\n"
"v_cvt_pk_bf16_f32 v147, v210, v211\n"
"s_waitcnt lgkmcnt(5)\n"
"v_lshlrev_b32_e32 v72, 16, v48\n"
"v_and_b32_e32 v73, s54, v48\n"
"v_lshlrev_b32_e32 v74, 16, v49\n"
"v_and_b32_e32 v75, s54, v49\n"
"v_lshlrev_b32_e32 v76, 16, v50\n"
"v_and_b32_e32 v77, s54, v50\n"
"v_lshlrev_b32_e32 v78, 16, v51\n"
"v_and_b32_e32 v79, s54, v51\n"
"v_pk_mul_f32 v[72:73], v[72:73], s[52:53]\n"
"v_pk_mul_f32 v[74:75], v[74:75], s[52:53]\n"
"v_pk_mul_f32 v[76:77], v[76:77], s[52:53]\n"
"v_pk_mul_f32 v[78:79], v[78:79], s[52:53]\n"
"v_cvt_pk_bf16_f32 v48, v72, v73\n"
"v_cvt_pk_bf16_f32 v49, v74, v75\n"
"v_cvt_pk_bf16_f32 v50, v76, v77\n"
"v_cvt_pk_bf16_f32 v51, v78, v79\n"
"v_accvgpr_write_b32 a24, v48\n"
"v_accvgpr_write_b32 a25, v49\n"
"v_accvgpr_write_b32 a26, v50\n"
"v_accvgpr_write_b32 a27, v51\n"
"s_waitcnt lgkmcnt(4)\n"
"v_lshlrev_b32_e32 v72, 16, v52\n"
"v_and_b32_e32 v73, s54, v52\n"
"v_lshlrev_b32_e32 v74, 16, v53\n"
"v_and_b32_e32 v75, s54, v53\n"
"v_mfma_f32_16x16x32_bf16 v[132:135], a[120:123], v[48:51], 0\n"
"v_lshlrev_b32_e32 v76, 16, v54\n"
"v_and_b32_e32 v77, s54, v54\n"
"v_lshlrev_b32_e32 v78, 16, v55\n"
"v_and_b32_e32 v79, s54, v55\n"
"v_mfma_f32_16x16x32_bf16 v[196:199], a[144:147], v[48:51], 0\n"
"v_pk_mul_f32 v[72:73], v[72:73], s[52:53]\n"
"v_pk_mul_f32 v[74:75], v[74:75], s[52:53]\n"
"v_pk_mul_f32 v[76:77], v[76:77], s[52:53]\n"
"v_pk_mul_f32 v[78:79], v[78:79], s[52:53]\n"
"v_mfma_f32_16x16x32_bf16 v[148:151], a[168:171], v[48:51], 0\n"
"v_cvt_pk_bf16_f32 v52, v72, v73\n"
"v_cvt_pk_bf16_f32 v53, v74, v75\n"
"v_cvt_pk_bf16_f32 v54, v76, v77\n"
"v_cvt_pk_bf16_f32 v55, v78, v79\n"
"v_mfma_f32_16x16x32_bf16 v[212:215], a[192:195], v[48:51], 0\n"
"s_waitcnt lgkmcnt(3)\n"
"v_accvgpr_write_b32 a28, v52\n"
"v_accvgpr_write_b32 a29, v53\n"
"v_accvgpr_write_b32 a30, v54\n"
"v_accvgpr_write_b32 a31, v55\n"
"v_lshlrev_b32_e32 v72, 16, v56\n"
"v_and_b32_e32 v73, s54, v56\n"
"v_lshlrev_b32_e32 v74, 16, v57\n"
"v_and_b32_e32 v75, s54, v57\n"
"v_mfma_f32_16x16x32_bf16 v[132:135], a[124:127], v[52:55], v[132:135]\n"
"v_lshlrev_b32_e32 v76, 16, v58\n"
"v_and_b32_e32 v77, s54, v58\n"
"v_lshlrev_b32_e32 v78, 16, v59\n"
"v_and_b32_e32 v79, s54, v59\n"
"v_mfma_f32_16x16x32_bf16 v[196:199], a[148:151], v[52:55], v[196:199]\n"
"v_pk_mul_f32 v[72:73], v[72:73], s[52:53]\n"
"v_pk_mul_f32 v[74:75], v[74:75], s[52:53]\n"
"v_pk_mul_f32 v[76:77], v[76:77], s[52:53]\n"
"v_pk_mul_f32 v[78:79], v[78:79], s[52:53]\n"
"v_mfma_f32_16x16x32_bf16 v[148:151], a[172:175], v[52:55], v[148:151]\n"
"v_cvt_pk_bf16_f32 v56, v72, v73\n"
"v_cvt_pk_bf16_f32 v57, v74, v75\n"
"v_cvt_pk_bf16_f32 v58, v76, v77\n"
"v_cvt_pk_bf16_f32 v59, v78, v79\n"
"v_mfma_f32_16x16x32_bf16 v[212:215], a[196:199], v[52:55], v[212:215]\n"
"s_waitcnt lgkmcnt(2)\n"
"v_accvgpr_write_b32 a32, v56\n"
"v_accvgpr_write_b32 a33, v57\n"
"v_accvgpr_write_b32 a34, v58\n"
"v_accvgpr_write_b32 a35, v59\n"
"v_lshlrev_b32_e32 v72, 16, v60\n"
"v_and_b32_e32 v73, s54, v60\n"
"v_lshlrev_b32_e32 v74, 16, v61\n"
"v_and_b32_e32 v75, s54, v61\n"
"s_waitcnt vmcnt(6)\n"
"s_add_u32 s73, s76, 15360\n"
"s_add_u32 m0, s77, 125952\n"
"s_nop 0\n"
"buffer_load_dwordx4 v168, s[48:51], s73 offen offset:0 nt lds\n"
"buffer_load_dwordx4 v169, s[48:51], s73 offen offset:1024 nt lds\n"
"buffer_load_dwordx4 v170, s[48:51], s73 offen offset:2048 nt lds\n"
"v_mfma_f32_16x16x32_bf16 v[132:135], a[128:131], v[56:59], v[132:135]\n"
"v_lshlrev_b32_e32 v76, 16, v62\n"
"v_and_b32_e32 v77, s54, v62\n"
"v_lshlrev_b32_e32 v78, 16, v63\n"
"v_and_b32_e32 v79, s54, v63\n"
"v_mfma_f32_16x16x32_bf16 v[196:199], a[152:155], v[56:59], v[196:199]\n"
"v_pk_mul_f32 v[72:73], v[72:73], s[52:53]\n"
"v_pk_mul_f32 v[74:75], v[74:75], s[52:53]\n"
"v_pk_mul_f32 v[76:77], v[76:77], s[52:53]\n"
"v_pk_mul_f32 v[78:79], v[78:79], s[52:53]\n"
"v_mfma_f32_16x16x32_bf16 v[148:151], a[176:179], v[56:59], v[148:151]\n"
"v_cvt_pk_bf16_f32 v60, v72, v73\n"
"v_cvt_pk_bf16_f32 v61, v74, v75\n"
"v_cvt_pk_bf16_f32 v62, v76, v77\n"
"v_cvt_pk_bf16_f32 v63, v78, v79\n"
"v_mfma_f32_16x16x32_bf16 v[212:215], a[200:203], v[56:59], v[212:215]\n"
"s_waitcnt lgkmcnt(1)\n"
"v_accvgpr_write_b32 a36, v60\n"
"v_accvgpr_write_b32 a37, v61\n"
"v_accvgpr_write_b32 a38, v62\n"
"v_accvgpr_write_b32 a39, v63\n"
"v_lshlrev_b32_e32 v72, 16, v64\n"
"v_and_b32_e32 v73, s54, v64\n"
"v_lshlrev_b32_e32 v74, 16, v65\n"
"v_and_b32_e32 v75, s54, v65\n"
"v_mfma_f32_16x16x32_bf16 v[132:135], a[132:135], v[60:63], v[132:135]\n"
"v_lshlrev_b32_e32 v76, 16, v66\n"
"v_and_b32_e32 v77, s54, v66\n"
"v_lshlrev_b32_e32 v78, 16, v67\n"
"v_and_b32_e32 v79, s54, v67\n"
"v_mfma_f32_16x16x32_bf16 v[196:199], a[156:159], v[60:63], v[196:199]\n"
"v_pk_mul_f32 v[72:73], v[72:73], s[52:53]\n"
"v_pk_mul_f32 v[74:75], v[74:75], s[52:53]\n"
"v_pk_mul_f32 v[76:77], v[76:77], s[52:53]\n"
"v_pk_mul_f32 v[78:79], v[78:79], s[52:53]\n"
"v_mfma_f32_16x16x32_bf16 v[148:151], a[180:183], v[60:63], v[148:151]\n"
"v_cvt_pk_bf16_f32 v64, v72, v73\n"
"v_cvt_pk_bf16_f32 v65, v74, v75\n"
"v_cvt_pk_bf16_f32 v66, v76, v77\n"
"v_cvt_pk_bf16_f32 v67, v78, v79\n"
"v_mfma_f32_16x16x32_bf16 v[212:215], a[204:207], v[60:63], v[212:215]\n"
"s_waitcnt lgkmcnt(0)\n"
"v_accvgpr_write_b32 a40, v64\n"
"v_accvgpr_write_b32 a41, v65\n"
"v_accvgpr_write_b32 a42, v66\n"
"v_accvgpr_write_b32 a43, v67\n"
"v_lshlrev_b32_e32 v72, 16, v68\n"
"v_and_b32_e32 v73, s54, v68\n"
"v_lshlrev_b32_e32 v74, 16, v69\n"
"v_and_b32_e32 v75, s54, v69\n"
"v_mfma_f32_16x16x32_bf16 v[132:135], a[136:139], v[64:67], v[132:135]\n"
"v_lshlrev_b32_e32 v76, 16, v70\n"
"v_and_b32_e32 v77, s54, v70\n"
"v_lshlrev_b32_e32 v78, 16, v71\n"
"v_and_b32_e32 v79, s54, v71\n"
"v_mfma_f32_16x16x32_bf16 v[196:199], a[160:163], v[64:67], v[196:199]\n"
"v_pk_mul_f32 v[72:73], v[72:73], s[52:53]\n"
"v_pk_mul_f32 v[74:75], v[74:75], s[52:53]\n"
"v_pk_mul_f32 v[76:77], v[76:77], s[52:53]\n"
"v_pk_mul_f32 v[78:79], v[78:79], s[52:53]\n"
"v_mfma_f32_16x16x32_bf16 v[148:151], a[184:187], v[64:67], v[148:151]\n"
"v_cvt_pk_bf16_f32 v68, v72, v73\n"
"v_cvt_pk_bf16_f32 v69, v74, v75\n"
"v_cvt_pk_bf16_f32 v70, v76, v77\n"
"v_cvt_pk_bf16_f32 v71, v78, v79\n"
"v_mfma_f32_16x16x32_bf16 v[212:215], a[208:211], v[64:67], v[212:215]\n"
"v_accvgpr_write_b32 a44, v68\n"
"v_accvgpr_write_b32 a45, v69\n"
"v_accvgpr_write_b32 a46, v70\n"
"v_accvgpr_write_b32 a47, v71\n"
"v_mfma_f32_16x16x32_bf16 v[132:135], a[140:143], v[68:71], v[132:135]\n"
"v_mfma_f32_16x16x32_bf16 v[196:199], a[164:167], v[68:71], v[196:199]\n"
"v_mfma_f32_16x16x32_bf16 v[148:151], a[188:191], v[68:71], v[148:151]\n"
"v_mfma_f32_16x16x32_bf16 v[212:215], a[212:215], v[68:71], v[212:215]\n"
"s_waitcnt vmcnt(6)\n"
"s_add_u32 s73, s76, 18432\n"
"s_add_u32 m0, s77, 73728\n"
"s_nop 0\n"
"buffer_load_dwordx4 v165, s[48:51], s73 offen offset:0 nt lds\n"
"buffer_load_dwordx4 v166, s[48:51], s73 offen offset:1024 nt lds\n"
"buffer_load_dwordx4 v167, s[48:51], s73 offen offset:2048 nt lds\n"
"s_waitcnt vmcnt(9) lgkmcnt(0)\n"
"s_barrier\n"
"ds_read_b128 v[0:3], v179 offset:0\n"
"ds_read_b128 v[4:7], v180 offset:0\n"
"ds_read_b128 v[8:11], v179 offset:128\n"
"ds_read_b128 v[12:15], v180 offset:128\n"
"ds_read_b128 v[16:19], v179 offset:256\n"
"ds_read_b128 v[20:23], v180 offset:256\n"
"v_exp_f32_e32 v132, v132\n"
"v_exp_f32_e32 v133, v133\n"
"v_exp_f32_e32 v134, v134\n"
"v_exp_f32_e32 v135, v135\n"
"v_exp_f32_e32 v196, v196\n"
"v_exp_f32_e32 v197, v197\n"
"v_exp_f32_e32 v198, v198\n"
"v_exp_f32_e32 v199, v199\n"
"v_add_f32_e32 v250, v250, v132\n"
"v_add_f32_e32 v250, v250, v133\n"
"v_add_f32_e32 v250, v250, v134\n"
"v_add_f32_e32 v250, v250, v135\n"
"v_add_f32_e32 v250, v250, v196\n"
"v_add_f32_e32 v250, v250, v197\n"
"v_add_f32_e32 v250, v250, v198\n"
"v_add_f32_e32 v250, v250, v199\n"
"v_cvt_pk_bf16_f32 v132, v132, v133\n"
"v_cvt_pk_bf16_f32 v133, v134, v135\n"
"v_cvt_pk_bf16_f32 v134, v196, v197\n"
"v_cvt_pk_bf16_f32 v135, v198, v199\n"
"v_exp_f32_e32 v148, v148\n"
"v_exp_f32_e32 v149, v149\n"
"v_exp_f32_e32 v150, v150\n"
"v_exp_f32_e32 v151, v151\n"
"v_exp_f32_e32 v212, v212\n"
"v_exp_f32_e32 v213, v213\n"
"v_exp_f32_e32 v214, v214\n"
"v_exp_f32_e32 v215, v215\n"
"v_add_f32_e32 v250, v250, v148\n"
"v_add_f32_e32 v250, v250, v149\n"
"v_add_f32_e32 v250, v250, v150\n"
"v_add_f32_e32 v250, v250, v151\n"
"v_add_f32_e32 v250, v250, v212\n"
"v_add_f32_e32 v250, v250, v213\n"
"v_add_f32_e32 v250, v250, v214\n"
"v_add_f32_e32 v250, v250, v215\n"
"v_cvt_pk_bf16_f32 v148, v148, v149\n"
"v_cvt_pk_bf16_f32 v149, v150, v151\n"
"v_cvt_pk_bf16_f32 v150, v212, v213\n"
"v_cvt_pk_bf16_f32 v151, v214, v215\n"
"s_waitcnt lgkmcnt(5)\n"
"v_lshlrev_b32_e32 v24, 16, v0\n"
"v_and_b32_e32 v25, s54, v0\n"
"v_lshlrev_b32_e32 v26, 16, v1\n"
"v_and_b32_e32 v27, s54, v1\n"
"v_lshlrev_b32_e32 v28, 16, v2\n"
"v_and_b32_e32 v29, s54, v2\n"
"v_lshlrev_b32_e32 v30, 16, v3\n"
"v_and_b32_e32 v31, s54, v3\n"
"v_pk_mul_f32 v[24:25], v[24:25], s[52:53]\n"
"v_pk_mul_f32 v[26:27], v[26:27], s[52:53]\n"
"v_pk_mul_f32 v[28:29], v[28:29], s[52:53]\n"
"v_pk_mul_f32 v[30:31], v[30:31], s[52:53]\n"
"v_cvt_pk_bf16_f32 v0, v24, v25\n"
"v_cvt_pk_bf16_f32 v1, v26, v27\n"
"v_cvt_pk_bf16_f32 v2, v28, v29\n"
"v_cvt_pk_bf16_f32 v3, v30, v31\n"
"v_accvgpr_write_b32 a96, v0\n"
"v_accvgpr_write_b32 a97, v1\n"
"v_accvgpr_write_b32 a98, v2\n"
"v_accvgpr_write_b32 a99, v3\n"
"s_waitcnt lgkmcnt(4)\n"
"v_lshlrev_b32_e32 v24, 16, v4\n"
"v_and_b32_e32 v25, s54, v4\n"
"v_lshlrev_b32_e32 v26, 16, v5\n"
"v_and_b32_e32 v27, s54, v5\n"
"v_mfma_f32_16x16x32_bf16 v[224:227], a[120:123], v[0:3], 0\n"
"v_lshlrev_b32_e32 v28, 16, v6\n"
"v_and_b32_e32 v29, s54, v6\n"
"v_lshlrev_b32_e32 v30, 16, v7\n"
"v_and_b32_e32 v31, s54, v7\n"
"v_mfma_f32_16x16x32_bf16 v[232:235], a[144:147], v[0:3], 0\n"
"s_waitcnt lgkmcnt(3)\n"
"v_pk_mul_f32 v[24:25], v[24:25], s[52:53]\n"
"v_pk_mul_f32 v[26:27], v[26:27], s[52:53]\n"
"v_pk_mul_f32 v[28:29], v[28:29], s[52:53]\n"
"v_pk_mul_f32 v[30:31], v[30:31], s[52:53]\n"
"v_cvt_pk_bf16_f32 v4, v24, v25\n"
"v_cvt_pk_bf16_f32 v5, v26, v27\n"
"v_cvt_pk_bf16_f32 v6, v28, v29\n"
"v_cvt_pk_bf16_f32 v7, v30, v31\n"
"v_accvgpr_write_b32 a100, v4\n"
"v_accvgpr_write_b32 a101, v5\n"
"v_accvgpr_write_b32 a102, v6\n"
"v_accvgpr_write_b32 a103, v7\n"
"v_lshlrev_b32_e32 v24, 16, v8\n"
"v_and_b32_e32 v25, s54, v8\n"
"v_lshlrev_b32_e32 v26, 16, v9\n"
"v_and_b32_e32 v27, s54, v9\n"
"v_mfma_f32_16x16x32_bf16 v[224:227], a[124:127], v[4:7], v[224:227]\n"
"v_lshlrev_b32_e32 v28, 16, v10\n"
"v_and_b32_e32 v29, s54, v10\n"
"v_lshlrev_b32_e32 v30, 16, v11\n"
"v_and_b32_e32 v31, s54, v11\n"
"v_mfma_f32_16x16x32_bf16 v[232:235], a[148:151], v[4:7], v[232:235]\n"
"s_waitcnt lgkmcnt(2)\n"
"v_pk_mul_f32 v[24:25], v[24:25], s[52:53]\n"
"v_pk_mul_f32 v[26:27], v[26:27], s[52:53]\n"
"v_pk_mul_f32 v[28:29], v[28:29], s[52:53]\n"
"v_pk_mul_f32 v[30:31], v[30:31], s[52:53]\n"
"v_cvt_pk_bf16_f32 v8, v24, v25\n"
"v_cvt_pk_bf16_f32 v9, v26, v27\n"
"v_cvt_pk_bf16_f32 v10, v28, v29\n"
"v_cvt_pk_bf16_f32 v11, v30, v31\n"
"v_accvgpr_write_b32 a104, v8\n"
"v_accvgpr_write_b32 a105, v9\n"
"v_accvgpr_write_b32 a106, v10\n"
"v_accvgpr_write_b32 a107, v11\n"
"v_lshlrev_b32_e32 v24, 16, v12\n"
"v_and_b32_e32 v25, s54, v12\n"
"v_lshlrev_b32_e32 v26, 16, v13\n"
"v_and_b32_e32 v27, s54, v13\n"
"v_mfma_f32_16x16x32_bf16 v[224:227], a[128:131], v[8:11], v[224:227]\n"
"v_lshlrev_b32_e32 v28, 16, v14\n"
"v_and_b32_e32 v29, s54, v14\n"
"v_lshlrev_b32_e32 v30, 16, v15\n"
"v_and_b32_e32 v31, s54, v15\n"
"v_mfma_f32_16x16x32_bf16 v[232:235], a[152:155], v[8:11], v[232:235]\n"
"s_waitcnt lgkmcnt(1)\n"
"v_pk_mul_f32 v[24:25], v[24:25], s[52:53]\n"
"v_pk_mul_f32 v[26:27], v[26:27], s[52:53]\n"
"v_pk_mul_f32 v[28:29], v[28:29], s[52:53]\n"
"v_pk_mul_f32 v[30:31], v[30:31], s[52:53]\n"
"v_cvt_pk_bf16_f32 v12, v24, v25\n"
"v_cvt_pk_bf16_f32 v13, v26, v27\n"
"v_cvt_pk_bf16_f32 v14, v28, v29\n"
"v_cvt_pk_bf16_f32 v15, v30, v31\n"
"v_accvgpr_write_b32 a108, v12\n"
"v_accvgpr_write_b32 a109, v13\n"
"v_accvgpr_write_b32 a110, v14\n"
"v_accvgpr_write_b32 a111, v15\n"
"v_lshlrev_b32_e32 v24, 16, v16\n"
"v_and_b32_e32 v25, s54, v16\n"
"v_lshlrev_b32_e32 v26, 16, v17\n"
"v_and_b32_e32 v27, s54, v17\n"
"v_mfma_f32_16x16x32_bf16 v[224:227], a[132:135], v[12:15], v[224:227]\n"
"v_lshlrev_b32_e32 v28, 16, v18\n"
"v_and_b32_e32 v29, s54, v18\n"
"v_lshlrev_b32_e32 v30, 16, v19\n"
"v_and_b32_e32 v31, s54, v19\n"
"v_mfma_f32_16x16x32_bf16 v[232:235], a[156:159], v[12:15], v[232:235]\n"
"s_waitcnt lgkmcnt(0)\n"
"v_pk_mul_f32 v[24:25], v[24:25], s[52:53]\n"
"v_pk_mul_f32 v[26:27], v[26:27], s[52:53]\n"
"v_pk_mul_f32 v[28:29], v[28:29], s[52:53]\n"
"v_pk_mul_f32 v[30:31], v[30:31], s[52:53]\n"
"v_cvt_pk_bf16_f32 v16, v24, v25\n"
"v_cvt_pk_bf16_f32 v17, v26, v27\n"
"v_cvt_pk_bf16_f32 v18, v28, v29\n"
"v_cvt_pk_bf16_f32 v19, v30, v31\n"
"v_accvgpr_write_b32 a112, v16\n"
"v_accvgpr_write_b32 a113, v17\n"
"v_accvgpr_write_b32 a114, v18\n"
"v_accvgpr_write_b32 a115, v19\n"
"v_lshlrev_b32_e32 v24, 16, v20\n"
"v_and_b32_e32 v25, s54, v20\n"
"v_lshlrev_b32_e32 v26, 16, v21\n"
"v_and_b32_e32 v27, s54, v21\n"
"v_mfma_f32_16x16x32_bf16 v[224:227], a[136:139], v[16:19], v[224:227]\n"
"v_lshlrev_b32_e32 v28, 16, v22\n"
"v_and_b32_e32 v29, s54, v22\n"
"v_lshlrev_b32_e32 v30, 16, v23\n"
"v_and_b32_e32 v31, s54, v23\n"
"v_mfma_f32_16x16x32_bf16 v[232:235], a[160:163], v[16:19], v[232:235]\n"
"v_pk_mul_f32 v[24:25], v[24:25], s[52:53]\n"
"v_pk_mul_f32 v[26:27], v[26:27], s[52:53]\n"
"v_pk_mul_f32 v[28:29], v[28:29], s[52:53]\n"
"v_pk_mul_f32 v[30:31], v[30:31], s[52:53]\n"
"v_cvt_pk_bf16_f32 v20, v24, v25\n"
"v_cvt_pk_bf16_f32 v21, v26, v27\n"
"v_cvt_pk_bf16_f32 v22, v28, v29\n"
"v_cvt_pk_bf16_f32 v23, v30, v31\n"
"v_accvgpr_write_b32 a116, v20\n"
"v_accvgpr_write_b32 a117, v21\n"
"v_accvgpr_write_b32 a118, v22\n"
"v_accvgpr_write_b32 a119, v23\n"
"v_mfma_f32_16x16x32_bf16 v[224:227], a[140:143], v[20:23], v[224:227]\n"
"v_mfma_f32_16x16x32_bf16 v[232:235], a[164:167], v[20:23], v[232:235]\n"
"s_waitcnt vmcnt(6)\n"
"s_add_u32 s73, s76, 21504\n"
"s_add_u32 m0, s77, 76800\n"
"s_nop 0\n"
"buffer_load_dwordx4 v168, s[48:51], s73 offen offset:0 nt lds\n"
"buffer_load_dwordx4 v169, s[48:51], s73 offen offset:1024 nt lds\n"
"buffer_load_dwordx4 v170, s[48:51], s73 offen offset:2048 nt lds\n"
"s_waitcnt vmcnt(6)\n"
"ds_read_b128 v[48:51], v177 offset:49152\n"
"ds_read_b128 v[52:55], v178 offset:49152\n"
"ds_read_b128 v[56:59], v177 offset:49280\n"
"ds_read_b128 v[60:63], v178 offset:49280\n"
"ds_read_b128 v[64:67], v177 offset:49408\n"
"ds_read_b128 v[68:71], v178 offset:49408\n"
"v_exp_f32_e32 v224, v224\n"
"v_exp_f32_e32 v225, v225\n"
"v_exp_f32_e32 v226, v226\n"
"v_exp_f32_e32 v227, v227\n"
"v_exp_f32_e32 v232, v232\n"
"v_exp_f32_e32 v233, v233\n"
"v_exp_f32_e32 v234, v234\n"
"v_exp_f32_e32 v235, v235\n"
"v_add_f32_e32 v253, v253, v224\n"
"v_add_f32_e32 v253, v253, v225\n"
"v_add_f32_e32 v253, v253, v226\n"
"v_add_f32_e32 v253, v253, v227\n"
"v_add_f32_e32 v253, v253, v232\n"
"v_add_f32_e32 v253, v253, v233\n"
"v_add_f32_e32 v253, v253, v234\n"
"v_add_f32_e32 v253, v253, v235\n"
"v_cvt_pk_bf16_f32 v224, v224, v225\n"
"v_cvt_pk_bf16_f32 v225, v226, v227\n"
"v_cvt_pk_bf16_f32 v226, v232, v233\n"
"v_cvt_pk_bf16_f32 v227, v234, v235\n"
"s_waitcnt lgkmcnt(5)\n"
"v_lshlrev_b32_e32 v72, 16, v48\n"
"v_and_b32_e32 v73, s54, v48\n"
"v_lshlrev_b32_e32 v74, 16, v49\n"
"v_and_b32_e32 v75, s54, v49\n"
"v_lshlrev_b32_e32 v76, 16, v50\n"
"v_and_b32_e32 v77, s54, v50\n"
"v_lshlrev_b32_e32 v78, 16, v51\n"
"v_and_b32_e32 v79, s54, v51\n"
"v_pk_mul_f32 v[72:73], v[72:73], s[52:53]\n"
"v_pk_mul_f32 v[74:75], v[74:75], s[52:53]\n"
"v_pk_mul_f32 v[76:77], v[76:77], s[52:53]\n"
"v_pk_mul_f32 v[78:79], v[78:79], s[52:53]\n"
"v_cvt_pk_bf16_f32 v48, v72, v73\n"
"v_cvt_pk_bf16_f32 v49, v74, v75\n"
"v_cvt_pk_bf16_f32 v50, v76, v77\n"
"v_cvt_pk_bf16_f32 v51, v78, v79\n"
"v_accvgpr_write_b32 a48, v48\n"
"v_accvgpr_write_b32 a49, v49\n"
"v_accvgpr_write_b32 a50, v50\n"
"v_accvgpr_write_b32 a51, v51\n"
"s_waitcnt lgkmcnt(4)\n"
"v_lshlrev_b32_e32 v72, 16, v52\n"
"v_and_b32_e32 v73, s54, v52\n"
"v_lshlrev_b32_e32 v74, 16, v53\n"
"v_and_b32_e32 v75, s54, v53\n"
"v_mfma_f32_16x16x32_bf16 v[136:139], a[120:123], v[48:51], 0\n"
"v_lshlrev_b32_e32 v76, 16, v54\n"
"v_and_b32_e32 v77, s54, v54\n"
"v_lshlrev_b32_e32 v78, 16, v55\n"
"v_and_b32_e32 v79, s54, v55\n"
"v_mfma_f32_16x16x32_bf16 v[200:203], a[144:147], v[48:51], 0\n"
"v_pk_mul_f32 v[72:73], v[72:73], s[52:53]\n"
"v_pk_mul_f32 v[74:75], v[74:75], s[52:53]\n"
"v_pk_mul_f32 v[76:77], v[76:77], s[52:53]\n"
"v_pk_mul_f32 v[78:79], v[78:79], s[52:53]\n"
"v_mfma_f32_16x16x32_bf16 v[152:155], a[168:171], v[48:51], 0\n"
"v_cvt_pk_bf16_f32 v52, v72, v73\n"
"v_cvt_pk_bf16_f32 v53, v74, v75\n"
"v_cvt_pk_bf16_f32 v54, v76, v77\n"
"v_cvt_pk_bf16_f32 v55, v78, v79\n"
"v_mfma_f32_16x16x32_bf16 v[216:219], a[192:195], v[48:51], 0\n"
"s_waitcnt lgkmcnt(3)\n"
"v_accvgpr_write_b32 a52, v52\n"
"v_accvgpr_write_b32 a53, v53\n"
"v_accvgpr_write_b32 a54, v54\n"
"v_accvgpr_write_b32 a55, v55\n"
"v_lshlrev_b32_e32 v72, 16, v56\n"
"v_and_b32_e32 v73, s54, v56\n"
"v_lshlrev_b32_e32 v74, 16, v57\n"
"v_and_b32_e32 v75, s54, v57\n"
"v_mfma_f32_16x16x32_bf16 v[136:139], a[124:127], v[52:55], v[136:139]\n"
"v_lshlrev_b32_e32 v76, 16, v58\n"
"v_and_b32_e32 v77, s54, v58\n"
"v_lshlrev_b32_e32 v78, 16, v59\n"
"v_and_b32_e32 v79, s54, v59\n"
"v_mfma_f32_16x16x32_bf16 v[200:203], a[148:151], v[52:55], v[200:203]\n"
"v_pk_mul_f32 v[72:73], v[72:73], s[52:53]\n"
"v_pk_mul_f32 v[74:75], v[74:75], s[52:53]\n"
"v_pk_mul_f32 v[76:77], v[76:77], s[52:53]\n"
"v_pk_mul_f32 v[78:79], v[78:79], s[52:53]\n"
"v_mfma_f32_16x16x32_bf16 v[152:155], a[172:175], v[52:55], v[152:155]\n"
"v_cvt_pk_bf16_f32 v56, v72, v73\n"
"v_cvt_pk_bf16_f32 v57, v74, v75\n"
"v_cvt_pk_bf16_f32 v58, v76, v77\n"
"v_cvt_pk_bf16_f32 v59, v78, v79\n"
"v_mfma_f32_16x16x32_bf16 v[216:219], a[196:199], v[52:55], v[216:219]\n"
"s_waitcnt lgkmcnt(2)\n"
"v_accvgpr_write_b32 a56, v56\n"
"v_accvgpr_write_b32 a57, v57\n"
"v_accvgpr_write_b32 a58, v58\n"
"v_accvgpr_write_b32 a59, v59\n"
"v_lshlrev_b32_e32 v72, 16, v60\n"
"v_and_b32_e32 v73, s54, v60\n"
"v_lshlrev_b32_e32 v74, 16, v61\n"
"v_and_b32_e32 v75, s54, v61\n"
"s_waitcnt vmcnt(6)\n"
"s_mov_b32 s73, 24576\n"
"s_add_u32 m0, s58, 24576\n"
"s_nop 0\n"
"buffer_load_dwordx4 v246, s[40:43], s73 offen offset:0 nt lds\n"
"buffer_load_dwordx4 v247, s[40:43], s73 offen offset:1024 nt lds\n"
"buffer_load_dwordx4 v248, s[40:43], s73 offen offset:2048 nt lds\n"
"v_mfma_f32_16x16x32_bf16 v[136:139], a[128:131], v[56:59], v[136:139]\n"
"v_lshlrev_b32_e32 v76, 16, v62\n"
"v_and_b32_e32 v77, s54, v62\n"
"v_lshlrev_b32_e32 v78, 16, v63\n"
"v_and_b32_e32 v79, s54, v63\n"
"v_mfma_f32_16x16x32_bf16 v[200:203], a[152:155], v[56:59], v[200:203]\n"
"v_pk_mul_f32 v[72:73], v[72:73], s[52:53]\n"
"v_pk_mul_f32 v[74:75], v[74:75], s[52:53]\n"
"v_pk_mul_f32 v[76:77], v[76:77], s[52:53]\n"
"v_pk_mul_f32 v[78:79], v[78:79], s[52:53]\n"
"v_mfma_f32_16x16x32_bf16 v[152:155], a[176:179], v[56:59], v[152:155]\n"
"v_cvt_pk_bf16_f32 v60, v72, v73\n"
"v_cvt_pk_bf16_f32 v61, v74, v75\n"
"v_cvt_pk_bf16_f32 v62, v76, v77\n"
"v_cvt_pk_bf16_f32 v63, v78, v79\n"
"v_mfma_f32_16x16x32_bf16 v[216:219], a[200:203], v[56:59], v[216:219]\n"
"s_waitcnt lgkmcnt(1)\n"
"v_accvgpr_write_b32 a60, v60\n"
"v_accvgpr_write_b32 a61, v61\n"
"v_accvgpr_write_b32 a62, v62\n"
"v_accvgpr_write_b32 a63, v63\n"
"v_lshlrev_b32_e32 v72, 16, v64\n"
"v_and_b32_e32 v73, s54, v64\n"
"v_lshlrev_b32_e32 v74, 16, v65\n"
"v_and_b32_e32 v75, s54, v65\n"
"v_mfma_f32_16x16x32_bf16 v[136:139], a[132:135], v[60:63], v[136:139]\n"
"v_lshlrev_b32_e32 v76, 16, v66\n"
"v_and_b32_e32 v77, s54, v66\n"
"v_lshlrev_b32_e32 v78, 16, v67\n"
"v_and_b32_e32 v79, s54, v67\n"
"v_mfma_f32_16x16x32_bf16 v[200:203], a[156:159], v[60:63], v[200:203]\n"
"v_pk_mul_f32 v[72:73], v[72:73], s[52:53]\n"
"v_pk_mul_f32 v[74:75], v[74:75], s[52:53]\n"
"v_pk_mul_f32 v[76:77], v[76:77], s[52:53]\n"
"v_pk_mul_f32 v[78:79], v[78:79], s[52:53]\n"
"v_mfma_f32_16x16x32_bf16 v[152:155], a[180:183], v[60:63], v[152:155]\n"
"v_cvt_pk_bf16_f32 v64, v72, v73\n"
"v_cvt_pk_bf16_f32 v65, v74, v75\n"
"v_cvt_pk_bf16_f32 v66, v76, v77\n"
"v_cvt_pk_bf16_f32 v67, v78, v79\n"
"v_mfma_f32_16x16x32_bf16 v[216:219], a[204:207], v[60:63], v[216:219]\n"
"s_waitcnt lgkmcnt(0)\n"
"v_accvgpr_write_b32 a64, v64\n"
"v_accvgpr_write_b32 a65, v65\n"
"v_accvgpr_write_b32 a66, v66\n"
"v_accvgpr_write_b32 a67, v67\n"
"v_lshlrev_b32_e32 v72, 16, v68\n"
"v_and_b32_e32 v73, s54, v68\n"
"v_lshlrev_b32_e32 v74, 16, v69\n"
"v_and_b32_e32 v75, s54, v69\n"
"v_mfma_f32_16x16x32_bf16 v[136:139], a[136:139], v[64:67], v[136:139]\n"
"v_lshlrev_b32_e32 v76, 16, v70\n"
"v_and_b32_e32 v77, s54, v70\n"
"v_lshlrev_b32_e32 v78, 16, v71\n"
"v_and_b32_e32 v79, s54, v71\n"
"v_mfma_f32_16x16x32_bf16 v[200:203], a[160:163], v[64:67], v[200:203]\n"
"v_pk_mul_f32 v[72:73], v[72:73], s[52:53]\n"
"v_pk_mul_f32 v[74:75], v[74:75], s[52:53]\n"
"v_pk_mul_f32 v[76:77], v[76:77], s[52:53]\n"
"v_pk_mul_f32 v[78:79], v[78:79], s[52:53]\n"
"v_mfma_f32_16x16x32_bf16 v[152:155], a[184:187], v[64:67], v[152:155]\n"
"v_cvt_pk_bf16_f32 v68, v72, v73\n"
"v_cvt_pk_bf16_f32 v69, v74, v75\n"
"v_cvt_pk_bf16_f32 v70, v76, v77\n"
"v_cvt_pk_bf16_f32 v71, v78, v79\n"
"v_mfma_f32_16x16x32_bf16 v[216:219], a[208:211], v[64:67], v[216:219]\n"
"v_accvgpr_write_b32 a68, v68\n"
"v_accvgpr_write_b32 a69, v69\n"
"v_accvgpr_write_b32 a70, v70\n"
"v_accvgpr_write_b32 a71, v71\n"
"v_mfma_f32_16x16x32_bf16 v[136:139], a[140:143], v[68:71], v[136:139]\n"
"v_mfma_f32_16x16x32_bf16 v[200:203], a[164:167], v[68:71], v[200:203]\n"
"v_mfma_f32_16x16x32_bf16 v[152:155], a[188:191], v[68:71], v[152:155]\n"
"v_mfma_f32_16x16x32_bf16 v[216:219], a[212:215], v[68:71], v[216:219]\n"
"s_waitcnt vmcnt(6)\n"
"s_mov_b32 s73, 36864\n"
"s_add_u32 m0, s59, 24576\n"
"s_nop 0\n"
"buffer_load_dwordx4 v246, s[40:43], s73 offen offset:0 nt lds\n"
"buffer_load_dwordx4 v247, s[40:43], s73 offen offset:1024 nt lds\n"
"buffer_load_dwordx4 v248, s[40:43], s73 offen offset:2048 nt lds\n"
"s_waitcnt vmcnt(6)\n"
"ds_read_b128 v[0:3], v177 offset:0\n"
"ds_read_b128 v[4:7], v178 offset:0\n"
"ds_read_b128 v[8:11], v177 offset:128\n"
"ds_read_b128 v[12:15], v178 offset:128\n"
"ds_read_b128 v[16:19], v177 offset:256\n"
"ds_read_b128 v[20:23], v178 offset:256\n"
"v_exp_f32_e32 v136, v136\n"
"v_exp_f32_e32 v137, v137\n"
"v_exp_f32_e32 v138, v138\n"
"v_exp_f32_e32 v139, v139\n"
"v_exp_f32_e32 v200, v200\n"
"v_exp_f32_e32 v201, v201\n"
"v_exp_f32_e32 v202, v202\n"
"v_exp_f32_e32 v203, v203\n"
"v_add_f32_e32 v251, v251, v136\n"
"v_add_f32_e32 v251, v251, v137\n"
"v_add_f32_e32 v251, v251, v138\n"
"v_add_f32_e32 v251, v251, v139\n"
"v_add_f32_e32 v251, v251, v200\n"
"v_add_f32_e32 v251, v251, v201\n"
"v_add_f32_e32 v251, v251, v202\n"
"v_add_f32_e32 v251, v251, v203\n"
"v_cvt_pk_bf16_f32 v136, v136, v137\n"
"v_cvt_pk_bf16_f32 v137, v138, v139\n"
"v_cvt_pk_bf16_f32 v138, v200, v201\n"
"v_cvt_pk_bf16_f32 v139, v202, v203\n"
"v_exp_f32_e32 v152, v152\n"
"v_exp_f32_e32 v153, v153\n"
"v_exp_f32_e32 v154, v154\n"
"v_exp_f32_e32 v155, v155\n"
"v_exp_f32_e32 v216, v216\n"
"v_exp_f32_e32 v217, v217\n"
"v_exp_f32_e32 v218, v218\n"
"v_exp_f32_e32 v219, v219\n"
"v_add_f32_e32 v251, v251, v152\n"
"v_add_f32_e32 v251, v251, v153\n"
"v_add_f32_e32 v251, v251, v154\n"
"v_add_f32_e32 v251, v251, v155\n"
"v_add_f32_e32 v251, v251, v216\n"
"v_add_f32_e32 v251, v251, v217\n"
"v_add_f32_e32 v251, v251, v218\n"
"v_add_f32_e32 v251, v251, v219\n"
"v_cvt_pk_bf16_f32 v152, v152, v153\n"
"v_cvt_pk_bf16_f32 v153, v154, v155\n"
"v_cvt_pk_bf16_f32 v154, v216, v217\n"
"v_cvt_pk_bf16_f32 v155, v218, v219\n"
"s_waitcnt lgkmcnt(5)\n"
"v_lshlrev_b32_e32 v24, 16, v0\n"
"v_and_b32_e32 v25, s54, v0\n"
"v_lshlrev_b32_e32 v26, 16, v1\n"
"v_and_b32_e32 v27, s54, v1\n"
"v_lshlrev_b32_e32 v28, 16, v2\n"
"v_and_b32_e32 v29, s54, v2\n"
"v_lshlrev_b32_e32 v30, 16, v3\n"
"v_and_b32_e32 v31, s54, v3\n"
"v_pk_mul_f32 v[24:25], v[24:25], s[52:53]\n"
"v_pk_mul_f32 v[26:27], v[26:27], s[52:53]\n"
"v_pk_mul_f32 v[28:29], v[28:29], s[52:53]\n"
"v_pk_mul_f32 v[30:31], v[30:31], s[52:53]\n"
"v_cvt_pk_bf16_f32 v0, v24, v25\n"
"v_cvt_pk_bf16_f32 v1, v26, v27\n"
"v_cvt_pk_bf16_f32 v2, v28, v29\n"
"v_cvt_pk_bf16_f32 v3, v30, v31\n"
"v_accvgpr_write_b32 a72, v0\n"
"v_accvgpr_write_b32 a73, v1\n"
"v_accvgpr_write_b32 a74, v2\n"
"v_accvgpr_write_b32 a75, v3\n"
"s_waitcnt lgkmcnt(4)\n"
"v_lshlrev_b32_e32 v24, 16, v4\n"
"v_and_b32_e32 v25, s54, v4\n"
"v_lshlrev_b32_e32 v26, 16, v5\n"
"v_and_b32_e32 v27, s54, v5\n"
"v_mfma_f32_16x16x32_bf16 v[140:143], a[120:123], v[0:3], 0\n"
"v_lshlrev_b32_e32 v28, 16, v6\n"
"v_and_b32_e32 v29, s54, v6\n"
"v_lshlrev_b32_e32 v30, 16, v7\n"
"v_and_b32_e32 v31, s54, v7\n"
"v_mfma_f32_16x16x32_bf16 v[204:207], a[144:147], v[0:3], 0\n"
"v_pk_mul_f32 v[24:25], v[24:25], s[52:53]\n"
"v_pk_mul_f32 v[26:27], v[26:27], s[52:53]\n"
"v_pk_mul_f32 v[28:29], v[28:29], s[52:53]\n"
"v_pk_mul_f32 v[30:31], v[30:31], s[52:53]\n"
"v_mfma_f32_16x16x32_bf16 v[156:159], a[168:171], v[0:3], 0\n"
"v_cvt_pk_bf16_f32 v4, v24, v25\n"
"v_cvt_pk_bf16_f32 v5, v26, v27\n"
"v_cvt_pk_bf16_f32 v6, v28, v29\n"
"v_cvt_pk_bf16_f32 v7, v30, v31\n"
"v_mfma_f32_16x16x32_bf16 v[220:223], a[192:195], v[0:3], 0\n"
"s_waitcnt lgkmcnt(3)\n"
"v_accvgpr_write_b32 a76, v4\n"
"v_accvgpr_write_b32 a77, v5\n"
"v_accvgpr_write_b32 a78, v6\n"
"v_accvgpr_write_b32 a79, v7\n"
"v_lshlrev_b32_e32 v24, 16, v8\n"
"v_and_b32_e32 v25, s54, v8\n"
"v_lshlrev_b32_e32 v26, 16, v9\n"
"v_and_b32_e32 v27, s54, v9\n"
"v_mfma_f32_16x16x32_bf16 v[140:143], a[124:127], v[4:7], v[140:143]\n"
"v_lshlrev_b32_e32 v28, 16, v10\n"
"v_and_b32_e32 v29, s54, v10\n"
"v_lshlrev_b32_e32 v30, 16, v11\n"
"v_and_b32_e32 v31, s54, v11\n"
"v_mfma_f32_16x16x32_bf16 v[204:207], a[148:151], v[4:7], v[204:207]\n"
"v_pk_mul_f32 v[24:25], v[24:25], s[52:53]\n"
"v_pk_mul_f32 v[26:27], v[26:27], s[52:53]\n"
"v_pk_mul_f32 v[28:29], v[28:29], s[52:53]\n"
"v_pk_mul_f32 v[30:31], v[30:31], s[52:53]\n"
"v_mfma_f32_16x16x32_bf16 v[156:159], a[172:175], v[4:7], v[156:159]\n"
"v_cvt_pk_bf16_f32 v8, v24, v25\n"
"v_cvt_pk_bf16_f32 v9, v26, v27\n"
"v_cvt_pk_bf16_f32 v10, v28, v29\n"
"v_cvt_pk_bf16_f32 v11, v30, v31\n"
"v_mfma_f32_16x16x32_bf16 v[220:223], a[196:199], v[4:7], v[220:223]\n"
"s_waitcnt lgkmcnt(2)\n"
"v_accvgpr_write_b32 a80, v8\n"
"v_accvgpr_write_b32 a81, v9\n"
"v_accvgpr_write_b32 a82, v10\n"
"v_accvgpr_write_b32 a83, v11\n"
"v_lshlrev_b32_e32 v24, 16, v12\n"
"v_and_b32_e32 v25, s54, v12\n"
"v_lshlrev_b32_e32 v26, 16, v13\n"
"v_and_b32_e32 v27, s54, v13\n"
"s_waitcnt vmcnt(6)\n"
"s_mov_b32 s73, 49152\n"
"s_add_u32 m0, s60, 24576\n"
"s_nop 0\n"
"buffer_load_dwordx4 v246, s[44:47], s73 offen offset:0 nt lds\n"
"buffer_load_dwordx4 v247, s[44:47], s73 offen offset:1024 nt lds\n"
"buffer_load_dwordx4 v248, s[44:47], s73 offen offset:2048 nt lds\n"
"v_mfma_f32_16x16x32_bf16 v[140:143], a[128:131], v[8:11], v[140:143]\n"
"v_lshlrev_b32_e32 v28, 16, v14\n"
"v_and_b32_e32 v29, s54, v14\n"
"v_lshlrev_b32_e32 v30, 16, v15\n"
"v_and_b32_e32 v31, s54, v15\n"
"v_mfma_f32_16x16x32_bf16 v[204:207], a[152:155], v[8:11], v[204:207]\n"
"v_pk_mul_f32 v[24:25], v[24:25], s[52:53]\n"
"v_pk_mul_f32 v[26:27], v[26:27], s[52:53]\n"
"v_pk_mul_f32 v[28:29], v[28:29], s[52:53]\n"
"v_pk_mul_f32 v[30:31], v[30:31], s[52:53]\n"
"v_mfma_f32_16x16x32_bf16 v[156:159], a[176:179], v[8:11], v[156:159]\n"
"v_cvt_pk_bf16_f32 v12, v24, v25\n"
"v_cvt_pk_bf16_f32 v13, v26, v27\n"
"v_cvt_pk_bf16_f32 v14, v28, v29\n"
"v_cvt_pk_bf16_f32 v15, v30, v31\n"
"v_mfma_f32_16x16x32_bf16 v[220:223], a[200:203], v[8:11], v[220:223]\n"
"s_waitcnt lgkmcnt(1)\n"
"v_accvgpr_write_b32 a84, v12\n"
"v_accvgpr_write_b32 a85, v13\n"
"v_accvgpr_write_b32 a86, v14\n"
"v_accvgpr_write_b32 a87, v15\n"
"v_lshlrev_b32_e32 v24, 16, v16\n"
"v_and_b32_e32 v25, s54, v16\n"
"v_lshlrev_b32_e32 v26, 16, v17\n"
"v_and_b32_e32 v27, s54, v17\n"
"v_mfma_f32_16x16x32_bf16 v[140:143], a[132:135], v[12:15], v[140:143]\n"
"v_lshlrev_b32_e32 v28, 16, v18\n"
"v_and_b32_e32 v29, s54, v18\n"
"v_lshlrev_b32_e32 v30, 16, v19\n"
"v_and_b32_e32 v31, s54, v19\n"
"v_mfma_f32_16x16x32_bf16 v[204:207], a[156:159], v[12:15], v[204:207]\n"
"v_pk_mul_f32 v[24:25], v[24:25], s[52:53]\n"
"v_pk_mul_f32 v[26:27], v[26:27], s[52:53]\n"
"v_pk_mul_f32 v[28:29], v[28:29], s[52:53]\n"
"v_pk_mul_f32 v[30:31], v[30:31], s[52:53]\n"
"v_mfma_f32_16x16x32_bf16 v[156:159], a[180:183], v[12:15], v[156:159]\n"
"v_cvt_pk_bf16_f32 v16, v24, v25\n"
"v_cvt_pk_bf16_f32 v17, v26, v27\n"
"v_cvt_pk_bf16_f32 v18, v28, v29\n"
"v_cvt_pk_bf16_f32 v19, v30, v31\n"
"v_mfma_f32_16x16x32_bf16 v[220:223], a[204:207], v[12:15], v[220:223]\n"
"s_waitcnt lgkmcnt(0)\n"
"v_accvgpr_write_b32 a88, v16\n"
"v_accvgpr_write_b32 a89, v17\n"
"v_accvgpr_write_b32 a90, v18\n"
"v_accvgpr_write_b32 a91, v19\n"
"v_lshlrev_b32_e32 v24, 16, v20\n"
"v_and_b32_e32 v25, s54, v20\n"
"v_lshlrev_b32_e32 v26, 16, v21\n"
"v_and_b32_e32 v27, s54, v21\n"
"v_mfma_f32_16x16x32_bf16 v[140:143], a[136:139], v[16:19], v[140:143]\n"
"v_lshlrev_b32_e32 v28, 16, v22\n"
"v_and_b32_e32 v29, s54, v22\n"
"v_lshlrev_b32_e32 v30, 16, v23\n"
"v_and_b32_e32 v31, s54, v23\n"
"v_mfma_f32_16x16x32_bf16 v[204:207], a[160:163], v[16:19], v[204:207]\n"
"v_pk_mul_f32 v[24:25], v[24:25], s[52:53]\n"
"v_pk_mul_f32 v[26:27], v[26:27], s[52:53]\n"
"v_pk_mul_f32 v[28:29], v[28:29], s[52:53]\n"
"v_pk_mul_f32 v[30:31], v[30:31], s[52:53]\n"
"v_mfma_f32_16x16x32_bf16 v[156:159], a[184:187], v[16:19], v[156:159]\n"
"v_cvt_pk_bf16_f32 v20, v24, v25\n"
"v_cvt_pk_bf16_f32 v21, v26, v27\n"
"v_cvt_pk_bf16_f32 v22, v28, v29\n"
"v_cvt_pk_bf16_f32 v23, v30, v31\n"
"v_mfma_f32_16x16x32_bf16 v[220:223], a[208:211], v[16:19], v[220:223]\n"
"v_accvgpr_write_b32 a92, v20\n"
"v_accvgpr_write_b32 a93, v21\n"
"v_accvgpr_write_b32 a94, v22\n"
"v_accvgpr_write_b32 a95, v23\n"
"v_mfma_f32_16x16x32_bf16 v[140:143], a[140:143], v[20:23], v[140:143]\n"
"v_mfma_f32_16x16x32_bf16 v[204:207], a[164:167], v[20:23], v[204:207]\n"
"v_mfma_f32_16x16x32_bf16 v[156:159], a[188:191], v[20:23], v[156:159]\n"
"v_mfma_f32_16x16x32_bf16 v[220:223], a[212:215], v[20:23], v[220:223]\n"
"s_waitcnt vmcnt(6)\n"
"s_mov_b32 s73, 61440\n"
"s_add_u32 m0, s61, 24576\n"
"s_nop 0\n"
"buffer_load_dwordx4 v246, s[44:47], s73 offen offset:0 nt lds\n"
"buffer_load_dwordx4 v247, s[44:47], s73 offen offset:1024 nt lds\n"
"buffer_load_dwordx4 v248, s[44:47], s73 offen offset:2048 nt lds\n"
"s_getpc_b64 s[74:75]\n"
".Lcp0_%=:\n"
"s_add_u32 s74, s74, .Lloop_%=-.Lcp0_%=\n"
"s_addc_u32 s75, s75, 0\n"
"v_mbcnt_lo_u32_b32 v254, -1, 0\n"
"v_mbcnt_hi_u32_b32 v254, -1, v254\n"
"v_min_u32_e32 v254, 13, v254\n"
"v_lshlrev_b32_e32 v254, 7, v254\n"
"s_mul_i32 s73, %3, 1792\n"
"v_add_u32_e32 v254, s73, v254\n"
"global_load_dword v255, v254, s[74:75]\n"
"s_mov_b32 s76, 24576\n"
"s_mov_b32 s77, -49152\n"
"s_waitcnt vmcnt(7) lgkmcnt(0)\n"
"s_barrier\n"
"s_getpc_b64 s[74:75]\n"
".Lcp1_%=:\n"
"s_add_u32 s74, s74, .LF_%=-.Lcp1_%=\n"
"s_addc_u32 s75, s75, 0\n"
"v_mbcnt_lo_u32_b32 v254, -1, 0\n"
"v_mbcnt_hi_u32_b32 v254, -1, v254\n"
"v_min_u32_e32 v254, 11, v254\n"
"v_lshlrev_b32_e32 v254, 7, v254\n"
"s_mul_i32 s73, %3, 1536\n"
"v_add_u32_e32 v254, s73, v254\n"
"global_load_dword v255, v254, s[74:75]\n"
"ds_read_b128 a[232:235], v236 offset:0\n"
"ds_read_b128 a[236:239], v237 offset:0\n"
"ds_read_b128 a[240:243], v236 offset:128\n"
"ds_read_b128 a[244:247], v237 offset:128\n"
"v_exp_f32_e32 v140, v140\n"
"v_exp_f32_e32 v141, v141\n"
"v_exp_f32_e32 v142, v142\n"
"v_exp_f32_e32 v143, v143\n"
"v_exp_f32_e32 v204, v204\n"
"v_exp_f32_e32 v205, v205\n"
"v_exp_f32_e32 v206, v206\n"
"v_exp_f32_e32 v207, v207\n"
"v_add_f32_e32 v252, v252, v140\n"
"v_add_f32_e32 v252, v252, v141\n"
"v_add_f32_e32 v252, v252, v142\n"
"v_add_f32_e32 v252, v252, v143\n"
"v_add_f32_e32 v252, v252, v204\n"
"v_add_f32_e32 v252, v252, v205\n"
"v_add_f32_e32 v252, v252, v206\n"
"v_add_f32_e32 v252, v252, v207\n"
"v_cvt_pk_bf16_f32 v140, v140, v141\n"
"v_cvt_pk_bf16_f32 v141, v142, v143\n"
"v_cvt_pk_bf16_f32 v142, v204, v205\n"
"v_cvt_pk_bf16_f32 v143, v206, v207\n"
"v_exp_f32_e32 v156, v156\n"
"v_exp_f32_e32 v157, v157\n"
"v_exp_f32_e32 v158, v158\n"
"v_exp_f32_e32 v159, v159\n"
"v_exp_f32_e32 v220, v220\n"
"v_exp_f32_e32 v221, v221\n"
"v_exp_f32_e32 v222, v222\n"
"v_exp_f32_e32 v223, v223\n"
"v_add_f32_e32 v252, v252, v156\n"
"v_add_f32_e32 v252, v252, v157\n"
"v_add_f32_e32 v252, v252, v158\n"
"v_add_f32_e32 v252, v252, v159\n"
"v_add_f32_e32 v252, v252, v220\n"
"v_add_f32_e32 v252, v252, v221\n"
"v_add_f32_e32 v252, v252, v222\n"
"v_add_f32_e32 v252, v252, v223\n"
"v_cvt_pk_bf16_f32 v156, v156, v157\n"
"v_cvt_pk_bf16_f32 v157, v158, v159\n"
"v_cvt_pk_bf16_f32 v158, v220, v221\n"
"v_cvt_pk_bf16_f32 v159, v222, v223\n"
".LB0_%=:\n"
"s_waitcnt lgkmcnt(2)\n"
"v_mfma_f32_16x16x32_bf16 v[160:163], a[232:235], a[0:3], 0\n"
"ds_read_b128 a[248:251], v236 offset:256\n"
"v_mfma_f32_16x16x32_bf16 v[160:163], a[236:239], a[4:7], v[160:163]\n"
"s_cmp_eq_u32 s57, 24576\n"
"v_mfma_f32_16x16x32_bf16 v[164:167], a[236:239], a[28:31], 0\n"
"s_cselect_b32 s63, s66, s65\n"
"v_mfma_f32_16x16x32_bf16 v[164:167], a[232:235], a[24:27], v[164:167]\n"
"s_cselect_b32 s64, s77, s76\n"
"v_mfma_f32_16x16x32_bf16 v[168:171], a[232:235], a[48:51], 0\n"
"v_mfma_f32_16x16x32_bf16 v[168:171], a[236:239], a[52:55], v[168:171]\n"
"ds_read_b128 a[252:255], v237 offset:256\n"
"v_mfma_f32_16x16x32_bf16 v[172:175], a[236:239], a[76:79], 0\n"
"s_mov_b32 s73, 49152\n"
"v_mfma_f32_16x16x32_bf16 v[172:175], a[232:235], a[72:75], v[172:175]\n"
"s_add_u32 m0, s58, 49152\n"
"v_mfma_f32_16x16x32_bf16 v[228:231], a[232:235], a[96:99], 0\n"
"s_waitcnt vmcnt(8)\n"
"buffer_load_dwordx4 v246, s[40:43], s73 offen offset:0 nt lds\n"
"v_mfma_f32_16x16x32_bf16 v[228:231], a[236:239], a[100:103], v[228:231]\n"
"s_waitcnt lgkmcnt(2)\n"
"v_mfma_f32_16x16x32_bf16 v[160:163], a[240:243], a[8:11], v[160:163]\n"
"ds_read_b128 a[232:235], v236 offset:6144\n"
"v_mfma_f32_16x16x32_bf16 v[160:163], a[244:247], a[12:15], v[160:163]\n"
"v_mfma_f32_16x16x32_bf16 v[164:167], a[244:247], a[36:39], v[164:167]\n"
"v_mfma_f32_16x16x32_bf16 v[164:167], a[240:243], a[32:35], v[164:167]\n"
"v_mfma_f32_16x16x32_bf16 v[168:171], a[240:243], a[56:59], v[168:171]\n"
"v_mfma_f32_16x16x32_bf16 v[168:171], a[244:247], a[60:63], v[168:171]\n"
"ds_read_b128 a[236:239], v237 offset:6144\n"
"v_mfma_f32_16x16x32_bf16 v[172:175], a[244:247], a[84:87], v[172:175]\n"
"s_waitcnt vmcnt(8)\n"
"buffer_load_dwordx4 v247, s[40:43], s73 offen offset:1024 nt lds\n"
"v_mfma_f32_16x16x32_bf16 v[172:175], a[240:243], a[80:83], v[172:175]\n"
"v_mfma_f32_16x16x32_bf16 v[228:231], a[240:243], a[104:107], v[228:231]\n"
"v_mfma_f32_16x16x32_bf16 v[228:231], a[244:247], a[108:111], v[228:231]\n"
"s_waitcnt lgkmcnt(2)\n"
"v_mfma_f32_16x16x32_bf16 v[160:163], a[248:251], a[16:19], v[160:163]\n"
"ds_read_b128 a[240:243], v236 offset:6272\n"
"v_mfma_f32_16x16x32_bf16 v[160:163], a[252:255], a[20:23], v[160:163]\n"
"v_mfma_f32_16x16x32_bf16 v[164:167], a[252:255], a[44:47], v[164:167]\n"
"v_mfma_f32_16x16x32_bf16 v[164:167], a[248:251], a[40:43], v[164:167]\n"
"v_mfma_f32_16x16x32_bf16 v[168:171], a[248:251], a[64:67], v[168:171]\n"
"s_waitcnt vmcnt(8)\n"
"buffer_load_dwordx4 v248, s[40:43], s73 offen offset:2048 nt lds\n"
"v_mfma_f32_16x16x32_bf16 v[168:171], a[252:255], a[68:71], v[168:171]\n"
"ds_read_b128 a[244:247], v237 offset:6272\n"
"v_mfma_f32_16x16x32_bf16 v[172:175], a[252:255], a[92:95], v[172:175]\n"
"v_mfma_f32_16x16x32_bf16 v[172:175], a[248:251], a[88:91], v[172:175]\n"
"v_mfma_f32_16x16x32_bf16 v[228:231], a[248:251], a[112:115], v[228:231]\n"
"v_mfma_f32_16x16x32_bf16 v[228:231], a[252:255], a[116:119], v[228:231]\n"
"s_waitcnt lgkmcnt(2)\n"
"v_exp_f32_e32 v160, v160\n"
"v_mfma_f32_16x16x32_bf16 v[192:195], a[232:235], a[0:3], 0\n"
"s_mov_b32 s73, 61440\n"
"ds_read_b128 a[248:251], v236 offset:6400\n"
"v_add_f32_e32 v249, v249, v160\n"
"v_mfma_f32_16x16x32_bf16 v[192:195], a[236:239], a[4:7], v[192:195]\n"
"s_add_u32 m0, s59, 49152\n"
"v_add_u32_e32 v236, s62, v236\n"
"v_mfma_f32_16x16x32_bf16 v[196:199], a[236:239], a[28:31], 0\n"
"s_waitcnt vmcnt(8)\n"
"buffer_load_dwordx4 v246, s[40:43], s73 offen offset:0 nt lds\n"
"v_exp_f32_e32 v161, v161\n"
"v_mfma_f32_16x16x32_bf16 v[196:199], a[232:235], a[24:27], v[196:199]\n"
"v_cvt_pk_bf16_f32 v160, v160, v161\n"
"v_add_f32_e32 v249, v249, v161\n"
"v_mfma_f32_16x16x32_bf16 v[200:203], a[232:235], a[48:51], 0\n"
"v_exp_f32_e32 v162, v162\n"
"v_mfma_f32_16x16x32_bf16 v[200:203], a[236:239], a[52:55], v[200:203]\n"
"ds_read_b128 a[252:255], v237 offset:6400\n"
"v_add_f32_e32 v249, v249, v162\n"
"v_mfma_f32_16x16x32_bf16 v[204:207], a[236:239], a[76:79], 0\n"
"v_add_u32_e32 v237, s62, v237\n"
"v_mfma_f32_16x16x32_bf16 v[204:207], a[232:235], a[72:75], v[204:207]\n"
"v_exp_f32_e32 v163, v163\n"
"v_mfma_f32_16x16x32_bf16 v[232:235], a[232:235], a[96:99], 0\n"
"v_cvt_pk_bf16_f32 v161, v162, v163\n"
"v_add_f32_e32 v249, v249, v163\n"
"v_mfma_f32_16x16x32_bf16 v[232:235], a[236:239], a[100:103], v[232:235]\n"
"s_waitcnt lgkmcnt(2)\n"
"v_exp_f32_e32 v164, v164\n"
"v_mfma_f32_16x16x32_bf16 v[192:195], a[240:243], a[8:11], v[192:195]\n"
"ds_read_b128 a[232:235], v236 offset:0\n"
"v_add_f32_e32 v250, v250, v164\n"
"v_mfma_f32_16x16x32_bf16 v[192:195], a[244:247], a[12:15], v[192:195]\n"
"s_waitcnt vmcnt(8)\n"
"buffer_load_dwordx4 v247, s[40:43], s73 offen offset:1024 nt lds\n"
"v_mfma_f32_16x16x32_bf16 v[196:199], a[244:247], a[36:39], v[196:199]\n"
"v_exp_f32_e32 v165, v165\n"
"v_mfma_f32_16x16x32_bf16 v[196:199], a[240:243], a[32:35], v[196:199]\n"
"v_cvt_pk_bf16_f32 v164, v164, v165\n"
"v_add_f32_e32 v250, v250, v165\n"
"v_mfma_f32_16x16x32_bf16 v[200:203], a[240:243], a[56:59], v[200:203]\n"
"v_exp_f32_e32 v166, v166\n"
"v_mfma_f32_16x16x32_bf16 v[200:203], a[244:247], a[60:63], v[200:203]\n"
"ds_read_b128 a[236:239], v237 offset:0\n"
"v_add_f32_e32 v250, v250, v166\n"
"v_mfma_f32_16x16x32_bf16 v[204:207], a[244:247], a[84:87], v[204:207]\n"
"v_exp_f32_e32 v167, v167\n"
"v_mfma_f32_16x16x32_bf16 v[204:207], a[240:243], a[80:83], v[204:207]\n"
"v_cvt_pk_bf16_f32 v165, v166, v167\n"
"v_add_f32_e32 v250, v250, v167\n"
"v_mfma_f32_16x16x32_bf16 v[232:235], a[240:243], a[104:107], v[232:235]\n"
"s_waitcnt vmcnt(8)\n"
"buffer_load_dwordx4 v248, s[40:43], s73 offen offset:2048 nt lds\n"
"v_exp_f32_e32 v168, v168\n"
"v_mfma_f32_16x16x32_bf16 v[232:235], a[244:247], a[108:111], v[232:235]\n"
"s_waitcnt lgkmcnt(2)\n"
"v_exp_f32_e32 v169, v169\n"
"v_mfma_f32_16x16x32_bf16 v[192:195], a[248:251], a[16:19], v[192:195]\n"
"ds_read_b128 a[240:243], v236 offset:128\n"
"v_add_f32_e32 v251, v251, v168\n"
"v_mfma_f32_16x16x32_bf16 v[192:195], a[252:255], a[20:23], v[192:195]\n"
"v_cvt_pk_bf16_f32 v168, v168, v169\n"
"v_add_f32_e32 v251, v251, v169\n"
"v_mfma_f32_16x16x32_bf16 v[196:199], a[252:255], a[44:47], v[196:199]\n"
"v_exp_f32_e32 v170, v170\n"
"v_mfma_f32_16x16x32_bf16 v[196:199], a[248:251], a[40:43], v[196:199]\n"
"v_exp_f32_e32 v171, v171\n"
"v_mfma_f32_16x16x32_bf16 v[200:203], a[248:251], a[64:67], v[200:203]\n"
"v_cvt_pk_bf16_f32 v169, v170, v171\n"
"v_add_f32_e32 v251, v251, v170\n"
"v_mfma_f32_16x16x32_bf16 v[200:203], a[252:255], a[68:71], v[200:203]\n"
"ds_read_b128 a[244:247], v237 offset:128\n"
"v_add_f32_e32 v251, v251, v171\n"
"v_mfma_f32_16x16x32_bf16 v[204:207], a[252:255], a[92:95], v[204:207]\n"
"v_mfma_f32_16x16x32_bf16 v[204:207], a[248:251], a[88:91], v[204:207]\n"
"v_exp_f32_e32 v172, v172\n"
"v_mfma_f32_16x16x32_bf16 v[232:235], a[248:251], a[112:115], v[232:235]\n"
"v_exp_f32_e32 v173, v173\n"
"v_mfma_f32_16x16x32_bf16 v[232:235], a[252:255], a[116:119], v[232:235]\n"
"s_waitcnt lgkmcnt(2)\n"
"v_add_f32_e32 v252, v252, v172\n"
"v_cvt_pk_bf16_f32 v172, v172, v173\n"
"v_mfma_f32_16x16x32_bf16 v[176:179], a[232:235], a[0:3], 0\n"
"ds_read_b128 a[248:251], v236 offset:256\n"
"v_add_f32_e32 v252, v252, v173\n"
"v_mfma_f32_16x16x32_bf16 v[176:179], a[236:239], a[4:7], v[176:179]\n"
"v_exp_f32_e32 v174, v174\n"
"v_mfma_f32_16x16x32_bf16 v[180:183], a[236:239], a[28:31], 0\n"
"v_exp_f32_e32 v175, v175\n"
"v_mfma_f32_16x16x32_bf16 v[180:183], a[232:235], a[24:27], v[180:183]\n"
"v_cvt_pk_bf16_f32 v173, v174, v175\n"
"v_add_f32_e32 v252, v252, v174\n"
"v_mfma_f32_16x16x32_bf16 v[184:187], a[232:235], a[48:51], 0\n"
"ds_read_b128 a[252:255], v237 offset:256\n"
"v_add_f32_e32 v252, v252, v175\n"
"v_mfma_f32_16x16x32_bf16 v[184:187], a[236:239], a[52:55], v[184:187]\n"
"v_exp_f32_e32 v228, v228\n"
"v_mfma_f32_16x16x32_bf16 v[188:191], a[236:239], a[76:79], 0\n"
"v_exp_f32_e32 v229, v229\n"
"v_mfma_f32_16x16x32_bf16 v[188:191], a[232:235], a[72:75], v[188:191]\n"
"s_waitcnt lgkmcnt(2)\n"
"v_add_f32_e32 v253, v253, v228\n"
"v_cvt_pk_bf16_f32 v228, v228, v229\n"
"v_mfma_f32_16x16x32_bf16 v[176:179], a[240:243], a[8:11], v[176:179]\n"
"ds_read_b128 a[232:235], v236 offset:6144\n"
"v_add_f32_e32 v253, v253, v229\n"
"v_mfma_f32_16x16x32_bf16 v[176:179], a[244:247], a[12:15], v[176:179]\n"
"v_exp_f32_e32 v230, v230\n"
"v_mfma_f32_16x16x32_bf16 v[180:183], a[244:247], a[36:39], v[180:183]\n"
"v_exp_f32_e32 v231, v231\n"
"v_mfma_f32_16x16x32_bf16 v[180:183], a[240:243], a[32:35], v[180:183]\n"
"v_cvt_pk_bf16_f32 v229, v230, v231\n"
"v_add_f32_e32 v253, v253, v230\n"
"v_mfma_f32_16x16x32_bf16 v[184:187], a[240:243], a[56:59], v[184:187]\n"
"ds_read_b128 a[236:239], v237 offset:6144\n"
"v_add_f32_e32 v253, v253, v231\n"
"v_mfma_f32_16x16x32_bf16 v[184:187], a[244:247], a[60:63], v[184:187]\n"
"v_mfma_f32_16x16x32_bf16 v[188:191], a[244:247], a[84:87], v[188:191]\n"
"v_exp_f32_e32 v192, v192\n"
"v_mfma_f32_16x16x32_bf16 v[188:191], a[240:243], a[80:83], v[188:191]\n"
"s_waitcnt lgkmcnt(2)\n"
"v_exp_f32_e32 v193, v193\n"
"v_mfma_f32_16x16x32_bf16 v[176:179], a[248:251], a[16:19], v[176:179]\n"
"ds_read_b128 a[240:243], v236 offset:6272\n"
"v_cvt_pk_bf16_f32 v162, v192, v193\n"
"v_mfma_f32_16x16x32_bf16 v[176:179], a[252:255], a[20:23], v[176:179]\n"
"v_add_f32_e32 v249, v249, v192\n"
"v_add_f32_e32 v249, v249, v193\n"
"v_mfma_f32_16x16x32_bf16 v[180:183], a[252:255], a[44:47], v[180:183]\n"
"v_exp_f32_e32 v194, v194\n"
"v_mfma_f32_16x16x32_bf16 v[180:183], a[248:251], a[40:43], v[180:183]\n"
"v_exp_f32_e32 v195, v195\n"
"v_mfma_f32_16x16x32_bf16 v[184:187], a[248:251], a[64:67], v[184:187]\n"
"ds_read_b128 a[244:247], v237 offset:6272\n"
"v_cvt_pk_bf16_f32 v163, v194, v195\n"
"v_mfma_f32_16x16x32_bf16 v[184:187], a[252:255], a[68:71], v[184:187]\n"
"v_add_f32_e32 v249, v249, v194\n"
"v_add_f32_e32 v249, v249, v195\n"
"v_mfma_f32_16x16x32_bf16 v[188:191], a[252:255], a[92:95], v[188:191]\n"
"v_exp_f32_e32 v196, v196\n"
"v_mfma_f32_16x16x32_bf16 v[188:191], a[248:251], a[88:91], v[188:191]\n"
"s_waitcnt lgkmcnt(2)\n"
"v_exp_f32_e32 v197, v197\n"
"v_mfma_f32_16x16x32_bf16 v[208:211], a[232:235], a[0:3], 0\n"
"ds_read_b128 a[248:251], v236 offset:6400\n"
"v_cvt_pk_bf16_f32 v166, v196, v197\n"
"v_mfma_f32_16x16x32_bf16 v[208:211], a[236:239], a[4:7], v[208:211]\n"
"v_add_f32_e32 v250, v250, v196\n"
"v_add_f32_e32 v250, v250, v197\n"
"v_mfma_f32_16x16x32_bf16 v[212:215], a[236:239], a[28:31], 0\n"
"v_exp_f32_e32 v198, v198\n"
"v_mfma_f32_16x16x32_bf16 v[212:215], a[232:235], a[24:27], v[212:215]\n"
"v_exp_f32_e32 v199, v199\n"
"v_mfma_f32_16x16x32_bf16 v[216:219], a[232:235], a[48:51], 0\n"
"ds_read_b128 a[252:255], v237 offset:6400\n"
"v_cvt_pk_bf16_f32 v167, v198, v199\n"
"v_mfma_f32_16x16x32_bf16 v[216:219], a[236:239], a[52:55], v[216:219]\n"
"v_mfma_f32_16x16x32_bf16 v[220:223], a[236:239], a[76:79], 0\n"
"v_add_u32_e32 v236, s63, v236\n"
"v_add_u32_e32 v237, s63, v237\n"
"v_mfma_f32_16x16x32_bf16 v[220:223], a[232:235], a[72:75], v[220:223]\n"
"s_waitcnt vmcnt(8)\n"
"s_barrier\n"
"s_waitcnt lgkmcnt(2)\n"
"v_mfma_f32_16x16x32_bf16 v[208:211], a[240:243], a[8:11], v[208:211]\n"
"ds_read_b64_tr_b16 a[232:233], v238 offset:0\n"
"ds_read_b64_tr_b16 a[234:235], v238 offset:6144\n"
"v_mfma_f32_16x16x32_bf16 v[208:211], a[244:247], a[12:15], v[208:211]\n"
"v_add_f32_e32 v250, v250, v198\n"
"v_add_f32_e32 v250, v250, v199\n"
"v_mfma_f32_16x16x32_bf16 v[212:215], a[244:247], a[36:39], v[212:215]\n"
"v_exp_f32_e32 v200, v200\n"
"v_mfma_f32_16x16x32_bf16 v[212:215], a[240:243], a[32:35], v[212:215]\n"
"s_mov_b32 s73, 73728\n"
"v_exp_f32_e32 v201, v201\n"
"v_mfma_f32_16x16x32_bf16 v[216:219], a[240:243], a[56:59], v[216:219]\n"
"s_add_u32 m0, s60, 49152\n"
"ds_read_b64_tr_b16 a[236:237], v242 offset:0\n"
"ds_read_b64_tr_b16 a[238:239], v242 offset:6144\n"
"v_mfma_f32_16x16x32_bf16 v[216:219], a[244:247], a[60:63], v[216:219]\n"
"s_waitcnt vmcnt(8)\n"
"buffer_load_dwordx4 v246, s[44:47], s73 offen offset:0 nt lds\n"
"v_cvt_pk_bf16_f32 v170, v200, v201\n"
"v_add_f32_e32 v251, v251, v200\n"
"v_mfma_f32_16x16x32_bf16 v[220:223], a[244:247], a[84:87], v[220:223]\n"
"v_exp_f32_e32 v202, v202\n"
"v_mfma_f32_16x16x32_bf16 v[220:223], a[240:243], a[80:83], v[220:223]\n"
"s_waitcnt lgkmcnt(4)\n"
"v_add_f32_e32 v251, v251, v201\n"
"v_add_f32_e32 v251, v251, v202\n"
"v_mfma_f32_16x16x32_bf16 v[208:211], a[248:251], a[16:19], v[208:211]\n"
"ds_read_b64_tr_b16 a[240:241], v239 offset:0\n"
"ds_read_b64_tr_b16 a[242:243], v239 offset:6144\n"
"v_mfma_f32_16x16x32_bf16 v[208:211], a[252:255], a[20:23], v[208:211]\n"
"v_exp_f32_e32 v203, v203\n"
"v_mfma_f32_16x16x32_bf16 v[212:215], a[252:255], a[44:47], v[212:215]\n"
"v_cvt_pk_bf16_f32 v171, v202, v203\n"
"v_add_f32_e32 v251, v251, v203\n"
"v_mfma_f32_16x16x32_bf16 v[212:215], a[248:251], a[40:43], v[212:215]\n"
"v_exp_f32_e32 v204, v204\n"
"v_mfma_f32_16x16x32_bf16 v[216:219], a[248:251], a[64:67], v[216:219]\n"
"ds_read_b64_tr_b16 a[244:245], v243 offset:0\n"
"ds_read_b64_tr_b16 a[246:247], v243 offset:6144\n"
"v_mfma_f32_16x16x32_bf16 v[216:219], a[252:255], a[68:71], v[216:219]\n"
"s_waitcnt vmcnt(8)\n"
"buffer_load_dwordx4 v247, s[44:47], s73 offen offset:1024 nt lds\n"
"v_mfma_f32_16x16x32_bf16 v[220:223], a[252:255], a[92:95], v[220:223]\n"
"v_exp_f32_e32 v205, v205\n"
"v_mfma_f32_16x16x32_bf16 v[220:223], a[248:251], a[88:91], v[220:223]\n"
"s_waitcnt lgkmcnt(4)\n"
"v_cvt_pk_bf16_f32 v174, v204, v205\n"
"v_add_f32_e32 v252, v252, v204\n"
"v_mfma_f32_16x16x32_bf16 a[120:123], a[232:235], v[128:131], 0\n"
"ds_read_b64_tr_b16 a[248:249], v240 offset:0\n"
"ds_read_b64_tr_b16 a[250:251], v240 offset:6144\n"
"v_mfma_f32_16x16x32_bf16 a[120:123], a[236:239], v[144:147], a[120:123]\n"
"v_exp_f32_e32 v206, v206\n"
"v_mfma_f32_16x16x32_bf16 a[124:127], a[236:239], v[148:151], 0\n"
"v_add_f32_e32 v252, v252, v205\n"
"v_add_f32_e32 v252, v252, v206\n"
"v_mfma_f32_16x16x32_bf16 a[124:127], a[232:235], v[132:135], a[124:127]\n"
"v_exp_f32_e32 v207, v207\n"
"v_mfma_f32_16x16x32_bf16 v[0:3], a[232:235], v[136:139], 0\n"
"ds_read_b64_tr_b16 a[252:253], v244 offset:0\n"
"ds_read_b64_tr_b16 a[254:255], v244 offset:6144\n"
"v_mfma_f32_16x16x32_bf16 v[0:3], a[236:239], v[152:155], v[0:3]\n"
"s_waitcnt vmcnt(8)\n"
"buffer_load_dwordx4 v248, s[44:47], s73 offen offset:2048 nt lds\n"
"v_cvt_pk_bf16_f32 v175, v206, v207\n"
"v_add_f32_e32 v252, v252, v207\n"
"v_mfma_f32_16x16x32_bf16 v[32:35], a[236:239], v[156:159], 0\n"
"v_exp_f32_e32 v232, v232\n"
"v_mfma_f32_16x16x32_bf16 v[32:35], a[232:235], v[140:143], v[32:35]\n"
"v_exp_f32_e32 v233, v233\n"
"v_mfma_f32_16x16x32_bf16 v[80:83], a[232:235], v[224:227], 0\n"
"s_waitcnt lgkmcnt(4)\n"
"v_cvt_pk_bf16_f32 v230, v232, v233\n"
"v_add_f32_e32 v253, v253, v232\n"
"v_mfma_f32_16x16x32_bf16 a[128:131], a[240:243], v[128:131], 0\n"
"ds_read_b64_tr_b16 a[232:233], v241 offset:0\n"
"ds_read_b64_tr_b16 a[234:235], v241 offset:6144\n"
"v_mfma_f32_16x16x32_bf16 a[128:131], a[244:247], v[144:147], a[128:131]\n"
"s_mov_b32 s73, 86016\n"
"v_exp_f32_e32 v234, v234\n"
"v_mfma_f32_16x16x32_bf16 a[132:135], a[244:247], v[148:151], 0\n"
"s_add_u32 m0, s61, 49152\n"
"v_add_f32_e32 v253, v253, v233\n"
"v_add_f32_e32 v253, v253, v234\n"
"v_mfma_f32_16x16x32_bf16 a[132:135], a[240:243], v[132:135], a[132:135]\n"
"s_waitcnt vmcnt(8)\n"
"buffer_load_dwordx4 v246, s[44:47], s73 offen offset:0 nt lds\n"
"v_mfma_f32_16x16x32_bf16 v[4:7], a[240:243], v[136:139], 0\n"
"ds_read_b64_tr_b16 a[236:237], v245 offset:0\n"
"ds_read_b64_tr_b16 a[238:239], v245 offset:6144\n"
"v_mfma_f32_16x16x32_bf16 v[4:7], a[244:247], v[152:155], v[4:7]\n"
"v_exp_f32_e32 v235, v235\n"
"v_mfma_f32_16x16x32_bf16 v[36:39], a[244:247], v[156:159], 0\n"
"v_cvt_pk_bf16_f32 v231, v234, v235\n"
"v_add_f32_e32 v253, v253, v235\n"
"v_mfma_f32_16x16x32_bf16 v[36:39], a[240:243], v[140:143], v[36:39]\n"
"v_exp_f32_e32 v176, v176\n"
"v_mfma_f32_16x16x32_bf16 v[84:87], a[240:243], v[224:227], 0\n"
"s_waitcnt lgkmcnt(4)\n"
"v_exp_f32_e32 v177, v177\n"
"v_mfma_f32_16x16x32_bf16 a[136:139], a[248:251], v[128:131], 0\n"
"ds_read_b64_tr_b16 a[240:241], v238 offset:128\n"
"ds_read_b64_tr_b16 a[242:243], v238 offset:6272\n"
"v_mfma_f32_16x16x32_bf16 a[136:139], a[252:255], v[144:147], a[136:139]\n"
"v_add_f32_e32 v249, v249, v176\n"
"v_cvt_pk_bf16_f32 v176, v176, v177\n"
"v_mfma_f32_16x16x32_bf16 a[140:143], a[252:255], v[148:151], 0\n"
"s_waitcnt vmcnt(8)\n"
"buffer_load_dwordx4 v247, s[44:47], s73 offen offset:1024 nt lds\n"
"v_exp_f32_e32 v178, v178\n"
"v_mfma_f32_16x16x32_bf16 a[140:143], a[248:251], v[132:135], a[140:143]\n"
"v_add_f32_e32 v249, v249, v177\n"
"v_add_f32_e32 v249, v249, v178\n"
"v_mfma_f32_16x16x32_bf16 v[8:11], a[248:251], v[136:139], 0\n"
"ds_read_b64_tr_b16 a[244:245], v242 offset:128\n"
"ds_read_b64_tr_b16 a[246:247], v242 offset:6272\n"
"v_mfma_f32_16x16x32_bf16 v[8:11], a[252:255], v[152:155], v[8:11]\n"
"v_exp_f32_e32 v179, v179\n"
"v_mfma_f32_16x16x32_bf16 v[40:43], a[252:255], v[156:159], 0\n"
"v_cvt_pk_bf16_f32 v177, v178, v179\n"
"v_add_f32_e32 v249, v249, v179\n"
"v_mfma_f32_16x16x32_bf16 v[40:43], a[248:251], v[140:143], v[40:43]\n"
"v_exp_f32_e32 v180, v180\n"
"v_mfma_f32_16x16x32_bf16 v[88:91], a[248:251], v[224:227], 0\n"
"s_waitcnt lgkmcnt(4)\n"
"v_exp_f32_e32 v181, v181\n"
"v_mfma_f32_16x16x32_bf16 a[144:147], a[232:235], v[128:131], 0\n"
"ds_read_b64_tr_b16 a[248:249], v239 offset:128\n"
"ds_read_b64_tr_b16 a[250:251], v239 offset:6272\n"
"v_mfma_f32_16x16x32_bf16 a[144:147], a[236:239], v[144:147], a[144:147]\n"
"s_waitcnt vmcnt(8)\n"
"buffer_load_dwordx4 v248, s[44:47], s73 offen offset:2048 nt lds\n"
"v_mfma_f32_16x16x32_bf16 a[148:151], a[236:239], v[148:151], 0\n"
"v_add_f32_e32 v250, v250, v180\n"
"v_cvt_pk_bf16_f32 v180, v180, v181\n"
"v_mfma_f32_16x16x32_bf16 a[148:151], a[232:235], v[132:135], a[148:151]\n"
"v_exp_f32_e32 v182, v182\n"
"v_mfma_f32_16x16x32_bf16 v[12:15], a[232:235], v[136:139], 0\n"
"ds_read_b64_tr_b16 a[252:253], v243 offset:128\n"
"ds_read_b64_tr_b16 a[254:255], v243 offset:6272\n"
"v_mfma_f32_16x16x32_bf16 v[12:15], a[236:239], v[152:155], v[12:15]\n"
"v_add_f32_e32 v250, v250, v181\n"
"v_add_f32_e32 v250, v250, v182\n"
"v_mfma_f32_16x16x32_bf16 v[44:47], a[236:239], v[156:159], 0\n"
"v_exp_f32_e32 v183, v183\n"
"v_mfma_f32_16x16x32_bf16 v[44:47], a[232:235], v[140:143], v[44:47]\n"
"v_cvt_pk_bf16_f32 v181, v182, v183\n"
"v_add_f32_e32 v250, v250, v183\n"
"v_mfma_f32_16x16x32_bf16 v[92:95], a[232:235], v[224:227], 0\n"
"s_waitcnt lgkmcnt(4)\n"
"v_exp_f32_e32 v184, v184\n"
"v_mfma_f32_16x16x32_bf16 a[152:155], a[240:243], v[128:131], 0\n"
"s_mov_b32 s73, 73728\n"
"ds_read_b64_tr_b16 a[232:233], v240 offset:128\n"
"ds_read_b64_tr_b16 a[234:235], v240 offset:6272\n"
"v_mfma_f32_16x16x32_bf16 a[152:155], a[244:247], v[144:147], a[152:155]\n"
"s_add_u32 m0, s58, 0\n"
"v_exp_f32_e32 v185, v185\n"
"v_mfma_f32_16x16x32_bf16 a[156:159], a[244:247], v[148:151], 0\n"
"s_waitcnt vmcnt(8)\n"
"buffer_load_dwordx4 v246, s[40:43], s73 offen offset:0 nt lds\n"
"v_add_f32_e32 v251, v251, v184\n"
"v_cvt_pk_bf16_f32 v184, v184, v185\n"
"v_mfma_f32_16x16x32_bf16 a[156:159], a[240:243], v[132:135], a[156:159]\n"
"v_exp_f32_e32 v186, v186\n"
"v_mfma_f32_16x16x32_bf16 v[16:19], a[240:243], v[136:139], 0\n"
"ds_read_b64_tr_b16 a[236:237], v244 offset:128\n"
"ds_read_b64_tr_b16 a[238:239], v244 offset:6272\n"
"v_mfma_f32_16x16x32_bf16 v[16:19], a[244:247], v[152:155], v[16:19]\n"
"v_add_f32_e32 v251, v251, v185\n"
"v_add_f32_e32 v251, v251, v186\n"
"v_mfma_f32_16x16x32_bf16 v[48:51], a[244:247], v[156:159], 0\n"
"v_exp_f32_e32 v187, v187\n"
"v_mfma_f32_16x16x32_bf16 v[48:51], a[240:243], v[140:143], v[48:51]\n"
"v_cvt_pk_bf16_f32 v185, v186, v187\n"
"v_add_f32_e32 v251, v251, v187\n"
"v_mfma_f32_16x16x32_bf16 v[96:99], a[240:243], v[224:227], 0\n"
"s_waitcnt lgkmcnt(4)\n"
"v_exp_f32_e32 v188, v188\n"
"v_mfma_f32_16x16x32_bf16 a[160:163], a[248:251], v[128:131], 0\n"
"ds_read_b64_tr_b16 a[240:241], v241 offset:128\n"
"ds_read_b64_tr_b16 a[242:243], v241 offset:6272\n"
"v_mfma_f32_16x16x32_bf16 a[160:163], a[252:255], v[144:147], a[160:163]\n"
"s_waitcnt vmcnt(8)\n"
"buffer_load_dwordx4 v247, s[40:43], s73 offen offset:1024 nt lds\n"
"v_mfma_f32_16x16x32_bf16 a[164:167], a[252:255], v[148:151], 0\n"
"v_exp_f32_e32 v189, v189\n"
"v_mfma_f32_16x16x32_bf16 a[164:167], a[248:251], v[132:135], a[164:167]\n"
"v_add_f32_e32 v252, v252, v188\n"
"v_cvt_pk_bf16_f32 v188, v188, v189\n"
"v_mfma_f32_16x16x32_bf16 v[20:23], a[248:251], v[136:139], 0\n"
"ds_read_b64_tr_b16 a[244:245], v245 offset:128\n"
"ds_read_b64_tr_b16 a[246:247], v245 offset:6272\n"
"v_mfma_f32_16x16x32_bf16 v[20:23], a[252:255], v[152:155], v[20:23]\n"
"v_exp_f32_e32 v190, v190\n"
"v_mfma_f32_16x16x32_bf16 v[52:55], a[252:255], v[156:159], 0\n"
"v_add_f32_e32 v252, v252, v189\n"
"v_add_f32_e32 v252, v252, v190\n"
"v_mfma_f32_16x16x32_bf16 v[52:55], a[248:251], v[140:143], v[52:55]\n"
"v_exp_f32_e32 v191, v191\n"
"v_mfma_f32_16x16x32_bf16 v[100:103], a[248:251], v[224:227], 0\n"
"s_waitcnt lgkmcnt(4)\n"
"v_cvt_pk_bf16_f32 v189, v190, v191\n"
"v_add_f32_e32 v252, v252, v191\n"
"v_mfma_f32_16x16x32_bf16 a[168:171], a[232:235], v[128:131], 0\n"
"ds_read_b64_tr_b16 a[248:249], v238 offset:256\n"
"ds_read_b64_tr_b16 a[250:251], v238 offset:6400\n"
"v_mfma_f32_16x16x32_bf16 a[168:171], a[236:239], v[144:147], a[168:171]\n"
"s_waitcnt vmcnt(8)\n"
"buffer_load_dwordx4 v248, s[40:43], s73 offen offset:2048 nt lds\n"
"v_exp_f32_e32 v208, v208\n"
"v_mfma_f32_16x16x32_bf16 a[172:175], a[236:239], v[148:151], 0\n"
"v_exp_f32_e32 v209, v209\n"
"v_mfma_f32_16x16x32_bf16 a[172:175], a[232:235], v[132:135], a[172:175]\n"
"v_cvt_pk_bf16_f32 v178, v208, v209\n"
"v_add_f32_e32 v249, v249, v208\n"
"v_mfma_f32_16x16x32_bf16 v[24:27], a[232:235], v[136:139], 0\n"
"ds_read_b64_tr_b16 a[252:253], v242 offset:256\n"
"ds_read_b64_tr_b16 a[254:255], v242 offset:6400\n"
"v_mfma_f32_16x16x32_bf16 v[24:27], a[236:239], v[152:155], v[24:27]\n"
"v_exp_f32_e32 v210, v210\n"
"v_mfma_f32_16x16x32_bf16 v[56:59], a[236:239], v[156:159], 0\n"
"v_mfma_f32_16x16x32_bf16 v[56:59], a[232:235], v[140:143], v[56:59]\n"
"s_mov_b32 s73, 86016\n"
"v_add_f32_e32 v249, v249, v209\n"
"v_add_f32_e32 v249, v249, v210\n"
"v_mfma_f32_16x16x32_bf16 v[104:107], a[232:235], v[224:227], 0\n"
"s_waitcnt lgkmcnt(4)\n"
"v_exp_f32_e32 v211, v211\n"
"v_mfma_f32_16x16x32_bf16 a[176:179], a[240:243], v[128:131], 0\n"
"s_add_u32 m0, s59, 0\n"
"ds_read_b64_tr_b16 a[232:233], v239 offset:256\n"
"ds_read_b64_tr_b16 a[234:235], v239 offset:6400\n"
"v_mfma_f32_16x16x32_bf16 a[176:179], a[244:247], v[144:147], a[176:179]\n"
"s_waitcnt vmcnt(8)\n"
"buffer_load_dwordx4 v246, s[40:43], s73 offen offset:0 nt lds\n"
"v_cvt_pk_bf16_f32 v179, v210, v211\n"
"v_add_f32_e32 v249, v249, v211\n"
"v_mfma_f32_16x16x32_bf16 a[180:183], a[244:247], v[148:151], 0\n"
"v_exp_f32_e32 v212, v212\n"
"v_mfma_f32_16x16x32_bf16 a[180:183], a[240:243], v[132:135], a[180:183]\n"
"v_exp_f32_e32 v213, v213\n"
"v_mfma_f32_16x16x32_bf16 v[28:31], a[240:243], v[136:139], 0\n"
"ds_read_b64_tr_b16 a[236:237], v243 offset:256\n"
"ds_read_b64_tr_b16 a[238:239], v243 offset:6400\n"
"v_mfma_f32_16x16x32_bf16 v[28:31], a[244:247], v[152:155], v[28:31]\n"
"v_cvt_pk_bf16_f32 v182, v212, v213\n"
"v_add_f32_e32 v250, v250, v212\n"
"v_mfma_f32_16x16x32_bf16 v[60:63], a[244:247], v[156:159], 0\n"
"v_exp_f32_e32 v214, v214\n"
"v_mfma_f32_16x16x32_bf16 v[60:63], a[240:243], v[140:143], v[60:63]\n"
"s_waitcnt vmcnt(8)\n"
"buffer_load_dwordx4 v247, s[40:43], s73 offen offset:1024 nt lds\n"
"v_add_f32_e32 v250, v250, v213\n"
"v_add_f32_e32 v250, v250, v214\n"
"v_mfma_f32_16x16x32_bf16 v[108:111], a[240:243], v[224:227], 0\n"
"s_waitcnt lgkmcnt(4)\n"
"v_exp_f32_e32 v215, v215\n"
"v_mfma_f32_16x16x32_bf16 a[184:187], a[248:251], v[128:131], 0\n"
"ds_read_b64_tr_b16 a[240:241], v240 offset:256\n"
"ds_read_b64_tr_b16 a[242:243], v240 offset:6400\n"
"v_mfma_f32_16x16x32_bf16 a[184:187], a[252:255], v[144:147], a[184:187]\n"
"v_cvt_pk_bf16_f32 v183, v214, v215\n"
"v_add_f32_e32 v250, v250, v215\n"
"v_mfma_f32_16x16x32_bf16 a[188:191], a[252:255], v[148:151], 0\n"
"v_exp_f32_e32 v216, v216\n"
"v_mfma_f32_16x16x32_bf16 a[188:191], a[248:251], v[132:135], a[188:191]\n"
"v_exp_f32_e32 v217, v217\n"
"v_mfma_f32_16x16x32_bf16 a[216:219], a[248:251], v[136:139], 0\n"
"ds_read_b64_tr_b16 a[244:245], v244 offset:256\n"
"ds_read_b64_tr_b16 a[246:247], v244 offset:6400\n"
"v_mfma_f32_16x16x32_bf16 a[216:219], a[252:255], v[152:155], a[216:219]\n"
"v_mfma_f32_16x16x32_bf16 v[64:67], a[252:255], v[156:159], 0\n"
"s_add_u32 s55, s55, 24576\n"
"s_waitcnt vmcnt(8)\n"
"buffer_load_dwordx4 v248, s[40:43], s73 offen offset:2048 nt lds\n"
"v_cvt_pk_bf16_f32 v186, v216, v217\n"
"v_add_f32_e32 v251, v251, v216\n"
"v_mfma_f32_16x16x32_bf16 v[64:67], a[248:251], v[140:143], v[64:67]\n"
"s_add_u32 s56, s56, 24576\n"
"v_exp_f32_e32 v218, v218\n"
"v_mfma_f32_16x16x32_bf16 v[112:115], a[248:251], v[224:227], 0\n"
"s_waitcnt lgkmcnt(4)\n"
"v_add_f32_e32 v251, v251, v217\n"
"v_add_f32_e32 v251, v251, v218\n"
"v_mfma_f32_16x16x32_bf16 a[192:195], a[232:235], v[128:131], 0\n"
"s_mov_b32 s79, s57\n"
"ds_read_b64_tr_b16 a[248:249], v241 offset:256\n"
"ds_read_b64_tr_b16 a[250:251], v241 offset:6400\n"
"v_mfma_f32_16x16x32_bf16 a[192:195], a[236:239], v[144:147], a[192:195]\n"
"s_add_u32 s57, s57, 24576\n"
"ds_read_b64_tr_b16 a[252:253], v245 offset:256\n"
"ds_read_b64_tr_b16 a[254:255], v245 offset:6400\n"
"v_mfma_f32_16x16x32_bf16 a[196:199], a[236:239], v[148:151], 0\n"
"s_cmp_eq_u32 s57, 73728\n"
"v_add_u32_e32 v238, s64, v238\n"
"v_add_u32_e32 v239, s64, v239\n"
"v_mfma_f32_16x16x32_bf16 a[196:199], a[232:235], v[132:135], a[196:199]\n"
"s_cselect_b32 s57, 0, s57\n"
"v_add_u32_e32 v240, s64, v240\n"
"v_add_u32_e32 v241, s64, v241\n"
"v_mfma_f32_16x16x32_bf16 a[220:223], a[232:235], v[136:139], 0\n"
"v_add_u32_e32 v242, s64, v242\n"
"v_add_u32_e32 v243, s64, v243\n"
"v_mfma_f32_16x16x32_bf16 a[220:223], a[236:239], v[152:155], a[220:223]\n"
"v_add_u32_e32 v244, s64, v244\n"
"v_add_u32_e32 v245, s64, v245\n"
"v_mfma_f32_16x16x32_bf16 v[68:71], a[236:239], v[156:159], 0\n"
"v_exp_f32_e32 v219, v219\n"
"v_mfma_f32_16x16x32_bf16 v[68:71], a[232:235], v[140:143], v[68:71]\n"
"v_cvt_pk_bf16_f32 v187, v218, v219\n"
"v_add_f32_e32 v251, v251, v219\n"
"v_mfma_f32_16x16x32_bf16 v[116:119], a[232:235], v[224:227], 0\n"
"s_waitcnt vmcnt(12) lgkmcnt(0)\n"
"s_barrier\n"
"v_mfma_f32_16x16x32_bf16 a[200:203], a[240:243], v[128:131], 0\n"
"ds_read_b128 a[232:235], v236 offset:0\n"
"v_mfma_f32_16x16x32_bf16 a[200:203], a[244:247], v[144:147], a[200:203]\n"
"v_exp_f32_e32 v220, v220\n"
"v_mfma_f32_16x16x32_bf16 a[204:207], a[244:247], v[148:151], 0\n"
"v_exp_f32_e32 v221, v221\n"
"v_mfma_f32_16x16x32_bf16 a[204:207], a[240:243], v[132:135], a[204:207]\n"
"v_cvt_pk_bf16_f32 v190, v220, v221\n"
"v_add_f32_e32 v252, v252, v220\n"
"v_mfma_f32_16x16x32_bf16 a[224:227], a[240:243], v[136:139], 0\n"
"ds_read_b128 a[236:239], v237 offset:0\n"
"v_add_f32_e32 v252, v252, v221\n"
"v_mfma_f32_16x16x32_bf16 a[224:227], a[244:247], v[152:155], a[224:227]\n"
"v_exp_f32_e32 v222, v222\n"
"v_mfma_f32_16x16x32_bf16 v[72:75], a[244:247], v[156:159], 0\n"
"v_exp_f32_e32 v223, v223\n"
"v_mfma_f32_16x16x32_bf16 v[72:75], a[240:243], v[140:143], v[72:75]\n"
"v_cvt_pk_bf16_f32 v191, v222, v223\n"
"v_add_f32_e32 v252, v252, v222\n"
"v_mfma_f32_16x16x32_bf16 v[120:123], a[240:243], v[224:227], 0\n"
"v_add_f32_e32 v252, v252, v223\n"
"v_mfma_f32_16x16x32_bf16 a[208:211], a[248:251], v[128:131], 0\n"
"ds_read_b128 a[240:243], v236 offset:128\n"
"v_mfma_f32_16x16x32_bf16 a[208:211], a[252:255], v[144:147], a[208:211]\n"
"v_mfma_f32_16x16x32_bf16 a[212:215], a[252:255], v[148:151], 0\n"
"v_mfma_f32_16x16x32_bf16 a[212:215], a[248:251], v[132:135], a[212:215]\n"
"v_mfma_f32_16x16x32_bf16 a[228:231], a[248:251], v[136:139], 0\n"
"ds_read_b128 a[244:247], v237 offset:128\n"
"v_mfma_f32_16x16x32_bf16 a[228:231], a[252:255], v[152:155], a[228:231]\n"
"v_mfma_f32_16x16x32_bf16 v[76:79], a[252:255], v[156:159], 0\n"
"v_mfma_f32_16x16x32_bf16 v[76:79], a[248:251], v[140:143], v[76:79]\n"
"v_mfma_f32_16x16x32_bf16 v[124:127], a[248:251], v[224:227], 0\n"
".Lloop_%=:\n"
"s_waitcnt lgkmcnt(2)\n"
"v_mfma_f32_16x16x32_bf16 v[128:131], a[232:235], a[0:3], 0\n"
"ds_read_b128 a[248:251], v236 offset:256\n"
"v_mfma_f32_16x16x32_bf16 v[128:131], a[236:239], a[4:7], v[128:131]\n"
"s_cmp_eq_u32 s57, 24576\n"
"v_mfma_f32_16x16x32_bf16 v[132:135], a[236:239], a[28:31], 0\n"
"s_cselect_b32 s63, s66, s65\n"
"v_mfma_f32_16x16x32_bf16 v[132:135], a[232:235], a[24:27], v[132:135]\n"
"s_cselect_b32 s64, s77, s76\n"
"v_mfma_f32_16x16x32_bf16 v[136:139], a[232:235], a[48:51], 0\n"
"v_mfma_f32_16x16x32_bf16 v[136:139], a[236:239], a[52:55], v[136:139]\n"
"ds_read_b128 a[252:255], v237 offset:256\n"
"v_mfma_f32_16x16x32_bf16 v[140:143], a[236:239], a[76:79], 0\n"
"v_mfma_f32_16x16x32_bf16 v[140:143], a[232:235], a[72:75], v[140:143]\n"
"s_add_u32 m0, s79, s60\n"
"v_mfma_f32_16x16x32_bf16 v[224:227], a[232:235], a[96:99], 0\n"
"s_waitcnt vmcnt(6)\n"
"buffer_load_dwordx4 v246, s[44:47], s55 offen offset:0 nt lds\n"
"v_mfma_f32_16x16x32_bf16 v[224:227], a[236:239], a[100:103], v[224:227]\n"
"s_waitcnt lgkmcnt(2)\n"
"v_mfma_f32_16x16x32_bf16 v[128:131], a[240:243], a[8:11], v[128:131]\n"
"ds_read_b128 a[232:235], v236 offset:6144\n"
"v_mfma_f32_16x16x32_bf16 v[128:131], a[244:247], a[12:15], v[128:131]\n"
"v_mfma_f32_16x16x32_bf16 v[132:135], a[244:247], a[36:39], v[132:135]\n"
"v_mfma_f32_16x16x32_bf16 v[132:135], a[240:243], a[32:35], v[132:135]\n"
"v_mfma_f32_16x16x32_bf16 v[136:139], a[240:243], a[56:59], v[136:139]\n"
"v_mfma_f32_16x16x32_bf16 v[136:139], a[244:247], a[60:63], v[136:139]\n"
"ds_read_b128 a[236:239], v237 offset:6144\n"
"v_mfma_f32_16x16x32_bf16 v[140:143], a[244:247], a[84:87], v[140:143]\n"
"v_mfma_f32_16x16x32_bf16 v[140:143], a[240:243], a[80:83], v[140:143]\n"
"v_mfma_f32_16x16x32_bf16 v[224:227], a[240:243], a[104:107], v[224:227]\n"
"v_mfma_f32_16x16x32_bf16 v[224:227], a[244:247], a[108:111], v[224:227]\n"
"s_waitcnt lgkmcnt(2)\n"
"v_mfma_f32_16x16x32_bf16 v[128:131], a[248:251], a[16:19], v[128:131]\n"
"ds_read_b128 a[240:243], v236 offset:6272\n"
"v_mfma_f32_16x16x32_bf16 v[128:131], a[252:255], a[20:23], v[128:131]\n"
"v_mfma_f32_16x16x32_bf16 v[132:135], a[252:255], a[44:47], v[132:135]\n"
"v_mfma_f32_16x16x32_bf16 v[132:135], a[248:251], a[40:43], v[132:135]\n"
"v_mfma_f32_16x16x32_bf16 v[136:139], a[248:251], a[64:67], v[136:139]\n"
"s_waitcnt vmcnt(6)\n"
"buffer_load_dwordx4 v247, s[44:47], s55 offen offset:1024 nt lds\n"
"v_mfma_f32_16x16x32_bf16 v[136:139], a[252:255], a[68:71], v[136:139]\n"
"ds_read_b128 a[244:247], v237 offset:6272\n"
"v_mfma_f32_16x16x32_bf16 v[140:143], a[252:255], a[92:95], v[140:143]\n"
"v_mfma_f32_16x16x32_bf16 v[140:143], a[248:251], a[88:91], v[140:143]\n"
"v_mfma_f32_16x16x32_bf16 v[224:227], a[248:251], a[112:115], v[224:227]\n"
"v_mfma_f32_16x16x32_bf16 v[224:227], a[252:255], a[116:119], v[224:227]\n"
"s_waitcnt lgkmcnt(2)\n"
"v_exp_f32_e32 v128, v128\n"
"v_mfma_f32_16x16x32_bf16 v[192:195], a[232:235], a[0:3], 0\n"
"ds_read_b128 a[248:251], v236 offset:6400\n"
"v_add_f32_e32 v249, v249, v128\n"
"v_mfma_f32_16x16x32_bf16 v[192:195], a[236:239], a[4:7], v[192:195]\n"
"v_add_u32_e32 v236, s62, v236\n"
"v_mfma_f32_16x16x32_bf16 v[196:199], a[236:239], a[28:31], 0\n"
"v_exp_f32_e32 v129, v129\n"
"v_mfma_f32_16x16x32_bf16 v[196:199], a[232:235], a[24:27], v[196:199]\n"
"v_cvt_pk_bf16_f32 v128, v128, v129\n"
"v_add_f32_e32 v249, v249, v129\n"
"v_mfma_f32_16x16x32_bf16 v[200:203], a[232:235], a[48:51], 0\n"
"v_exp_f32_e32 v130, v130\n"
"v_mfma_f32_16x16x32_bf16 v[200:203], a[236:239], a[52:55], v[200:203]\n"
"ds_read_b128 a[252:255], v237 offset:6400\n"
"v_add_f32_e32 v249, v249, v130\n"
"v_mfma_f32_16x16x32_bf16 v[204:207], a[236:239], a[76:79], 0\n"
"v_add_u32_e32 v237, s62, v237\n"
"v_mfma_f32_16x16x32_bf16 v[204:207], a[232:235], a[72:75], v[204:207]\n"
"v_exp_f32_e32 v131, v131\n"
"v_mfma_f32_16x16x32_bf16 v[232:235], a[232:235], a[96:99], 0\n"
"v_cvt_pk_bf16_f32 v129, v130, v131\n"
"v_add_f32_e32 v249, v249, v131\n"
"v_mfma_f32_16x16x32_bf16 v[232:235], a[236:239], a[100:103], v[232:235]\n"
"s_waitcnt lgkmcnt(2)\n"
"v_exp_f32_e32 v132, v132\n"
"v_mfma_f32_16x16x32_bf16 v[192:195], a[240:243], a[8:11], v[192:195]\n"
"ds_read_b128 a[232:235], v236 offset:0\n"
"v_add_f32_e32 v250, v250, v132\n"
"v_mfma_f32_16x16x32_bf16 v[192:195], a[244:247], a[12:15], v[192:195]\n"
"s_waitcnt vmcnt(6)\n"
"buffer_load_dwordx4 v248, s[44:47], s55 offen offset:2048 nt lds\n"
"v_mfma_f32_16x16x32_bf16 v[196:199], a[244:247], a[36:39], v[196:199]\n"
"v_exp_f32_e32 v133, v133\n"
"v_mfma_f32_16x16x32_bf16 v[196:199], a[240:243], a[32:35], v[196:199]\n"
"v_cvt_pk_bf16_f32 v132, v132, v133\n"
"v_add_f32_e32 v250, v250, v133\n"
"v_mfma_f32_16x16x32_bf16 v[200:203], a[240:243], a[56:59], v[200:203]\n"
"v_exp_f32_e32 v134, v134\n"
"v_mfma_f32_16x16x32_bf16 v[200:203], a[244:247], a[60:63], v[200:203]\n"
"ds_read_b128 a[236:239], v237 offset:0\n"
"v_add_f32_e32 v250, v250, v134\n"
"v_mfma_f32_16x16x32_bf16 v[204:207], a[244:247], a[84:87], v[204:207]\n"
"v_exp_f32_e32 v135, v135\n"
"v_mfma_f32_16x16x32_bf16 v[204:207], a[240:243], a[80:83], v[204:207]\n"
"v_cvt_pk_bf16_f32 v133, v134, v135\n"
"v_add_f32_e32 v250, v250, v135\n"
"v_mfma_f32_16x16x32_bf16 v[232:235], a[240:243], a[104:107], v[232:235]\n"
"v_exp_f32_e32 v136, v136\n"
"v_mfma_f32_16x16x32_bf16 v[232:235], a[244:247], a[108:111], v[232:235]\n"
"s_waitcnt lgkmcnt(2)\n"
"v_exp_f32_e32 v137, v137\n"
"v_mfma_f32_16x16x32_bf16 v[192:195], a[248:251], a[16:19], v[192:195]\n"
"ds_read_b128 a[240:243], v236 offset:128\n"
"v_add_f32_e32 v251, v251, v136\n"
"v_mfma_f32_16x16x32_bf16 v[192:195], a[252:255], a[20:23], v[192:195]\n"
"v_cvt_pk_bf16_f32 v136, v136, v137\n"
"v_add_f32_e32 v251, v251, v137\n"
"v_mfma_f32_16x16x32_bf16 v[196:199], a[252:255], a[44:47], v[196:199]\n"
"v_exp_f32_e32 v138, v138\n"
"v_mfma_f32_16x16x32_bf16 v[196:199], a[248:251], a[40:43], v[196:199]\n"
"v_exp_f32_e32 v139, v139\n"
"v_mfma_f32_16x16x32_bf16 v[200:203], a[248:251], a[64:67], v[200:203]\n"
"v_cvt_pk_bf16_f32 v137, v138, v139\n"
"v_add_f32_e32 v251, v251, v138\n"
"v_mfma_f32_16x16x32_bf16 v[200:203], a[252:255], a[68:71], v[200:203]\n"
"s_add_u32 m0, s79, s61\n"
"ds_read_b128 a[244:247], v237 offset:128\n"
"v_add_f32_e32 v251, v251, v139\n"
"v_mfma_f32_16x16x32_bf16 v[204:207], a[252:255], a[92:95], v[204:207]\n"
"s_waitcnt vmcnt(6)\n"
"buffer_load_dwordx4 v246, s[44:47], s56 offen offset:0 nt lds\n"
"v_mfma_f32_16x16x32_bf16 v[204:207], a[248:251], a[88:91], v[204:207]\n"
"v_exp_f32_e32 v140, v140\n"
"v_mfma_f32_16x16x32_bf16 v[232:235], a[248:251], a[112:115], v[232:235]\n"
"v_exp_f32_e32 v141, v141\n"
"v_mfma_f32_16x16x32_bf16 v[232:235], a[252:255], a[116:119], v[232:235]\n"
"s_waitcnt lgkmcnt(2)\n"
"v_add_f32_e32 v252, v252, v140\n"
"v_cvt_pk_bf16_f32 v140, v140, v141\n"
"v_mfma_f32_16x16x32_bf16 v[144:147], a[232:235], a[0:3], 0\n"
"ds_read_b128 a[248:251], v236 offset:256\n"
"v_add_f32_e32 v252, v252, v141\n"
"v_mfma_f32_16x16x32_bf16 v[144:147], a[236:239], a[4:7], v[144:147]\n"
"v_exp_f32_e32 v142, v142\n"
"v_mfma_f32_16x16x32_bf16 v[148:151], a[236:239], a[28:31], 0\n"
"v_exp_f32_e32 v143, v143\n"
"v_mfma_f32_16x16x32_bf16 v[148:151], a[232:235], a[24:27], v[148:151]\n"
"v_cvt_pk_bf16_f32 v141, v142, v143\n"
"v_add_f32_e32 v252, v252, v142\n"
"v_mfma_f32_16x16x32_bf16 v[152:155], a[232:235], a[48:51], 0\n"
"ds_read_b128 a[252:255], v237 offset:256\n"
"v_add_f32_e32 v252, v252, v143\n"
"v_mfma_f32_16x16x32_bf16 v[152:155], a[236:239], a[52:55], v[152:155]\n"
"v_exp_f32_e32 v224, v224\n"
"v_mfma_f32_16x16x32_bf16 v[156:159], a[236:239], a[76:79], 0\n"
"v_exp_f32_e32 v225, v225\n"
"v_mfma_f32_16x16x32_bf16 v[156:159], a[232:235], a[72:75], v[156:159]\n"
"s_waitcnt lgkmcnt(2)\n"
"v_add_f32_e32 v253, v253, v224\n"
"v_cvt_pk_bf16_f32 v224, v224, v225\n"
"v_mfma_f32_16x16x32_bf16 v[144:147], a[240:243], a[8:11], v[144:147]\n"
"ds_read_b128 a[232:235], v236 offset:6144\n"
"v_add_f32_e32 v253, v253, v225\n"
"v_mfma_f32_16x16x32_bf16 v[144:147], a[244:247], a[12:15], v[144:147]\n"
"v_exp_f32_e32 v226, v226\n"
"v_mfma_f32_16x16x32_bf16 v[148:151], a[244:247], a[36:39], v[148:151]\n"
"v_exp_f32_e32 v227, v227\n"
"v_mfma_f32_16x16x32_bf16 v[148:151], a[240:243], a[32:35], v[148:151]\n"
"v_cvt_pk_bf16_f32 v225, v226, v227\n"
"v_add_f32_e32 v253, v253, v226\n"
"v_mfma_f32_16x16x32_bf16 v[152:155], a[240:243], a[56:59], v[152:155]\n"
"ds_read_b128 a[236:239], v237 offset:6144\n"
"v_add_f32_e32 v253, v253, v227\n"
"v_mfma_f32_16x16x32_bf16 v[152:155], a[244:247], a[60:63], v[152:155]\n"
"s_waitcnt vmcnt(6)\n"
"buffer_load_dwordx4 v247, s[44:47], s56 offen offset:1024 nt lds\n"
"v_mfma_f32_16x16x32_bf16 v[156:159], a[244:247], a[84:87], v[156:159]\n"
"v_exp_f32_e32 v192, v192\n"
"v_mfma_f32_16x16x32_bf16 v[156:159], a[240:243], a[80:83], v[156:159]\n"
"s_waitcnt lgkmcnt(2)\n"
"v_exp_f32_e32 v193, v193\n"
"v_mfma_f32_16x16x32_bf16 v[144:147], a[248:251], a[16:19], v[144:147]\n"
"ds_read_b128 a[240:243], v236 offset:6272\n"
"v_cvt_pk_bf16_f32 v130, v192, v193\n"
"v_mfma_f32_16x16x32_bf16 v[144:147], a[252:255], a[20:23], v[144:147]\n"
"v_add_f32_e32 v249, v249, v192\n"
"v_add_f32_e32 v249, v249, v193\n"
"v_mfma_f32_16x16x32_bf16 v[148:151], a[252:255], a[44:47], v[148:151]\n"
"v_exp_f32_e32 v194, v194\n"
"v_mfma_f32_16x16x32_bf16 v[148:151], a[248:251], a[40:43], v[148:151]\n"
"v_exp_f32_e32 v195, v195\n"
"v_mfma_f32_16x16x32_bf16 v[152:155], a[248:251], a[64:67], v[152:155]\n"
"ds_read_b128 a[244:247], v237 offset:6272\n"
"v_cvt_pk_bf16_f32 v131, v194, v195\n"
"v_mfma_f32_16x16x32_bf16 v[152:155], a[252:255], a[68:71], v[152:155]\n"
"v_add_f32_e32 v249, v249, v194\n"
"v_add_f32_e32 v249, v249, v195\n"
"v_mfma_f32_16x16x32_bf16 v[156:159], a[252:255], a[92:95], v[156:159]\n"
"v_exp_f32_e32 v196, v196\n"
"v_mfma_f32_16x16x32_bf16 v[156:159], a[248:251], a[88:91], v[156:159]\n"
"s_waitcnt lgkmcnt(2)\n"
"v_exp_f32_e32 v197, v197\n"
"v_mfma_f32_16x16x32_bf16 v[208:211], a[232:235], a[0:3], 0\n"
"ds_read_b128 a[248:251], v236 offset:6400\n"
"v_cvt_pk_bf16_f32 v134, v196, v197\n"
"v_mfma_f32_16x16x32_bf16 v[208:211], a[236:239], a[4:7], v[208:211]\n"
"v_add_f32_e32 v250, v250, v196\n"
"v_add_f32_e32 v250, v250, v197\n"
"v_mfma_f32_16x16x32_bf16 v[212:215], a[236:239], a[28:31], 0\n"
"v_exp_f32_e32 v198, v198\n"
"v_mfma_f32_16x16x32_bf16 v[212:215], a[232:235], a[24:27], v[212:215]\n"
"v_exp_f32_e32 v199, v199\n"
"v_mfma_f32_16x16x32_bf16 v[216:219], a[232:235], a[48:51], 0\n"
"ds_read_b128 a[252:255], v237 offset:6400\n"
"v_cvt_pk_bf16_f32 v135, v198, v199\n"
"v_mfma_f32_16x16x32_bf16 v[216:219], a[236:239], a[52:55], v[216:219]\n"
"s_waitcnt vmcnt(6)\n"
"buffer_load_dwordx4 v248, s[44:47], s56 offen offset:2048 nt lds\n"
"v_mfma_f32_16x16x32_bf16 v[220:223], a[236:239], a[76:79], 0\n"
"v_add_u32_e32 v236, s63, v236\n"
"v_add_u32_e32 v237, s63, v237\n"
"v_mfma_f32_16x16x32_bf16 v[220:223], a[232:235], a[72:75], v[220:223]\n"
"s_waitcnt vmcnt(12)\n"
"s_barrier\n"
"s_waitcnt lgkmcnt(2)\n"
"v_mfma_f32_16x16x32_bf16 v[208:211], a[240:243], a[8:11], v[208:211]\n"
"ds_read_b64_tr_b16 a[232:233], v238 offset:0\n"
"ds_read_b64_tr_b16 a[234:235], v238 offset:6144\n"
"v_mfma_f32_16x16x32_bf16 v[208:211], a[244:247], a[12:15], v[208:211]\n"
"v_add_f32_e32 v250, v250, v198\n"
"v_add_f32_e32 v250, v250, v199\n"
"v_mfma_f32_16x16x32_bf16 v[212:215], a[244:247], a[36:39], v[212:215]\n"
"v_exp_f32_e32 v200, v200\n"
"v_mfma_f32_16x16x32_bf16 v[212:215], a[240:243], a[32:35], v[212:215]\n"
"v_exp_f32_e32 v201, v201\n"
"v_mfma_f32_16x16x32_bf16 v[216:219], a[240:243], a[56:59], v[216:219]\n"
"ds_read_b64_tr_b16 a[236:237], v242 offset:0\n"
"ds_read_b64_tr_b16 a[238:239], v242 offset:6144\n"
"v_mfma_f32_16x16x32_bf16 v[216:219], a[244:247], a[60:63], v[216:219]\n"
"v_cvt_pk_bf16_f32 v138, v200, v201\n"
"v_add_f32_e32 v251, v251, v200\n"
"v_mfma_f32_16x16x32_bf16 v[220:223], a[244:247], a[84:87], v[220:223]\n"
"v_exp_f32_e32 v202, v202\n"
"v_mfma_f32_16x16x32_bf16 v[220:223], a[240:243], a[80:83], v[220:223]\n"
"s_waitcnt lgkmcnt(4)\n"
"v_add_f32_e32 v251, v251, v201\n"
"v_add_f32_e32 v251, v251, v202\n"
"v_mfma_f32_16x16x32_bf16 v[208:211], a[248:251], a[16:19], v[208:211]\n"
"ds_read_b64_tr_b16 a[240:241], v239 offset:0\n"
"ds_read_b64_tr_b16 a[242:243], v239 offset:6144\n"
"v_mfma_f32_16x16x32_bf16 v[208:211], a[252:255], a[20:23], v[208:211]\n"
"v_exp_f32_e32 v203, v203\n"
"v_mfma_f32_16x16x32_bf16 v[212:215], a[252:255], a[44:47], v[212:215]\n"
"v_cvt_pk_bf16_f32 v139, v202, v203\n"
"v_add_f32_e32 v251, v251, v203\n"
"v_mfma_f32_16x16x32_bf16 v[212:215], a[248:251], a[40:43], v[212:215]\n"
"v_exp_f32_e32 v204, v204\n"
"v_mfma_f32_16x16x32_bf16 v[216:219], a[248:251], a[64:67], v[216:219]\n"
"s_add_u32 m0, s57, s58\n"
"ds_read_b64_tr_b16 a[244:245], v243 offset:0\n"
"ds_read_b64_tr_b16 a[246:247], v243 offset:6144\n"
"v_mfma_f32_16x16x32_bf16 v[216:219], a[252:255], a[68:71], v[216:219]\n"
"s_waitcnt vmcnt(6)\n"
"buffer_load_dwordx4 v246, s[40:43], s55 offen offset:0 nt lds\n"
"v_mfma_f32_16x16x32_bf16 v[220:223], a[252:255], a[92:95], v[220:223]\n"
"v_exp_f32_e32 v205, v205\n"
"v_mfma_f32_16x16x32_bf16 v[220:223], a[248:251], a[88:91], v[220:223]\n"
"s_waitcnt lgkmcnt(4)\n"
"v_cvt_pk_bf16_f32 v142, v204, v205\n"
"v_add_f32_e32 v252, v252, v204\n"
"v_mfma_f32_16x16x32_bf16 a[120:123], a[232:235], v[160:163], a[120:123]\n"
"ds_read_b64_tr_b16 a[248:249], v240 offset:0\n"
"ds_read_b64_tr_b16 a[250:251], v240 offset:6144\n"
"v_mfma_f32_16x16x32_bf16 a[120:123], a[236:239], v[176:179], a[120:123]\n"
"v_exp_f32_e32 v206, v206\n"
"v_mfma_f32_16x16x32_bf16 a[124:127], a[236:239], v[180:183], a[124:127]\n"
"v_add_f32_e32 v252, v252, v205\n"
"v_add_f32_e32 v252, v252, v206\n"
"v_mfma_f32_16x16x32_bf16 a[124:127], a[232:235], v[164:167], a[124:127]\n"
"v_exp_f32_e32 v207, v207\n"
"v_mfma_f32_16x16x32_bf16 v[0:3], a[232:235], v[168:171], v[0:3]\n"
"ds_read_b64_tr_b16 a[252:253], v244 offset:0\n"
"ds_read_b64_tr_b16 a[254:255], v244 offset:6144\n"
"v_mfma_f32_16x16x32_bf16 v[0:3], a[236:239], v[184:187], v[0:3]\n"
"v_cvt_pk_bf16_f32 v143, v206, v207\n"
"v_add_f32_e32 v252, v252, v207\n"
"v_mfma_f32_16x16x32_bf16 v[32:35], a[236:239], v[188:191], v[32:35]\n"
"v_exp_f32_e32 v232, v232\n"
"v_mfma_f32_16x16x32_bf16 v[32:35], a[232:235], v[172:175], v[32:35]\n"
"v_exp_f32_e32 v233, v233\n"
"v_mfma_f32_16x16x32_bf16 v[80:83], a[232:235], v[228:231], v[80:83]\n"
"s_waitcnt lgkmcnt(4)\n"
"v_cvt_pk_bf16_f32 v226, v232, v233\n"
"v_add_f32_e32 v253, v253, v232\n"
"v_mfma_f32_16x16x32_bf16 a[128:131], a[240:243], v[160:163], a[128:131]\n"
"ds_read_b64_tr_b16 a[232:233], v241 offset:0\n"
"ds_read_b64_tr_b16 a[234:235], v241 offset:6144\n"
"v_mfma_f32_16x16x32_bf16 a[128:131], a[244:247], v[176:179], a[128:131]\n"
"v_exp_f32_e32 v234, v234\n"
"v_mfma_f32_16x16x32_bf16 a[132:135], a[244:247], v[180:183], a[132:135]\n"
"v_add_f32_e32 v253, v253, v233\n"
"v_add_f32_e32 v253, v253, v234\n"
"v_mfma_f32_16x16x32_bf16 a[132:135], a[240:243], v[164:167], a[132:135]\n"
"s_waitcnt vmcnt(6)\n"
"buffer_load_dwordx4 v247, s[40:43], s55 offen offset:1024 nt lds\n"
"v_mfma_f32_16x16x32_bf16 v[4:7], a[240:243], v[168:171], v[4:7]\n"
"ds_read_b64_tr_b16 a[236:237], v245 offset:0\n"
"ds_read_b64_tr_b16 a[238:239], v245 offset:6144\n"
"v_mfma_f32_16x16x32_bf16 v[4:7], a[244:247], v[184:187], v[4:7]\n"
"v_exp_f32_e32 v235, v235\n"
"v_mfma_f32_16x16x32_bf16 v[36:39], a[244:247], v[188:191], v[36:39]\n"
"v_cvt_pk_bf16_f32 v227, v234, v235\n"
"v_add_f32_e32 v253, v253, v235\n"
"v_mfma_f32_16x16x32_bf16 v[36:39], a[240:243], v[172:175], v[36:39]\n"
"v_exp_f32_e32 v144, v144\n"
"v_mfma_f32_16x16x32_bf16 v[84:87], a[240:243], v[228:231], v[84:87]\n"
"s_waitcnt lgkmcnt(4)\n"
"v_exp_f32_e32 v145, v145\n"
"v_mfma_f32_16x16x32_bf16 a[136:139], a[248:251], v[160:163], a[136:139]\n"
"ds_read_b64_tr_b16 a[240:241], v238 offset:128\n"
"ds_read_b64_tr_b16 a[242:243], v238 offset:6272\n"
"v_mfma_f32_16x16x32_bf16 a[136:139], a[252:255], v[176:179], a[136:139]\n"
"v_add_f32_e32 v249, v249, v144\n"
"v_cvt_pk_bf16_f32 v144, v144, v145\n"
"v_mfma_f32_16x16x32_bf16 a[140:143], a[252:255], v[180:183], a[140:143]\n"
"v_exp_f32_e32 v146, v146\n"
"v_mfma_f32_16x16x32_bf16 a[140:143], a[248:251], v[164:167], a[140:143]\n"
"v_add_f32_e32 v249, v249, v145\n"
"v_add_f32_e32 v249, v249, v146\n"
"v_mfma_f32_16x16x32_bf16 v[8:11], a[248:251], v[168:171], v[8:11]\n"
"ds_read_b64_tr_b16 a[244:245], v242 offset:128\n"
"ds_read_b64_tr_b16 a[246:247], v242 offset:6272\n"
"v_mfma_f32_16x16x32_bf16 v[8:11], a[252:255], v[184:187], v[8:11]\n"
"v_exp_f32_e32 v147, v147\n"
"v_mfma_f32_16x16x32_bf16 v[40:43], a[252:255], v[188:191], v[40:43]\n"
"v_cvt_pk_bf16_f32 v145, v146, v147\n"
"v_add_f32_e32 v249, v249, v147\n"
"v_mfma_f32_16x16x32_bf16 v[40:43], a[248:251], v[172:175], v[40:43]\n"
"v_exp_f32_e32 v148, v148\n"
"v_mfma_f32_16x16x32_bf16 v[88:91], a[248:251], v[228:231], v[88:91]\n"
"s_waitcnt lgkmcnt(4)\n"
"v_exp_f32_e32 v149, v149\n"
"v_mfma_f32_16x16x32_bf16 a[144:147], a[232:235], v[160:163], a[144:147]\n"
"ds_read_b64_tr_b16 a[248:249], v239 offset:128\n"
"ds_read_b64_tr_b16 a[250:251], v239 offset:6272\n"
"v_mfma_f32_16x16x32_bf16 a[144:147], a[236:239], v[176:179], a[144:147]\n"
"s_waitcnt vmcnt(6)\n"
"buffer_load_dwordx4 v248, s[40:43], s55 offen offset:2048 nt lds\n"
"v_mfma_f32_16x16x32_bf16 a[148:151], a[236:239], v[180:183], a[148:151]\n"
"v_add_f32_e32 v250, v250, v148\n"
"v_cvt_pk_bf16_f32 v148, v148, v149\n"
"v_mfma_f32_16x16x32_bf16 a[148:151], a[232:235], v[164:167], a[148:151]\n"
"v_exp_f32_e32 v150, v150\n"
"v_mfma_f32_16x16x32_bf16 v[12:15], a[232:235], v[168:171], v[12:15]\n"
"ds_read_b64_tr_b16 a[252:253], v243 offset:128\n"
"ds_read_b64_tr_b16 a[254:255], v243 offset:6272\n"
"v_mfma_f32_16x16x32_bf16 v[12:15], a[236:239], v[184:187], v[12:15]\n"
"v_add_f32_e32 v250, v250, v149\n"
"v_add_f32_e32 v250, v250, v150\n"
"v_mfma_f32_16x16x32_bf16 v[44:47], a[236:239], v[188:191], v[44:47]\n"
"v_exp_f32_e32 v151, v151\n"
"v_mfma_f32_16x16x32_bf16 v[44:47], a[232:235], v[172:175], v[44:47]\n"
"v_cvt_pk_bf16_f32 v149, v150, v151\n"
"v_add_f32_e32 v250, v250, v151\n"
"v_mfma_f32_16x16x32_bf16 v[92:95], a[232:235], v[228:231], v[92:95]\n"
"s_waitcnt lgkmcnt(4)\n"
"v_exp_f32_e32 v152, v152\n"
"v_mfma_f32_16x16x32_bf16 a[152:155], a[240:243], v[160:163], a[152:155]\n"
"ds_read_b64_tr_b16 a[232:233], v240 offset:128\n"
"ds_read_b64_tr_b16 a[234:235], v240 offset:6272\n"
"v_mfma_f32_16x16x32_bf16 a[152:155], a[244:247], v[176:179], a[152:155]\n"
"v_exp_f32_e32 v153, v153\n"
"v_mfma_f32_16x16x32_bf16 a[156:159], a[244:247], v[180:183], a[156:159]\n"
"v_add_f32_e32 v251, v251, v152\n"
"v_cvt_pk_bf16_f32 v152, v152, v153\n"
"v_mfma_f32_16x16x32_bf16 a[156:159], a[240:243], v[164:167], a[156:159]\n"
"v_exp_f32_e32 v154, v154\n"
"v_mfma_f32_16x16x32_bf16 v[16:19], a[240:243], v[168:171], v[16:19]\n"
"ds_read_b64_tr_b16 a[236:237], v244 offset:128\n"
"ds_read_b64_tr_b16 a[238:239], v244 offset:6272\n"
"v_mfma_f32_16x16x32_bf16 v[16:19], a[244:247], v[184:187], v[16:19]\n"
"v_add_f32_e32 v251, v251, v153\n"
"v_add_f32_e32 v251, v251, v154\n"
"v_mfma_f32_16x16x32_bf16 v[48:51], a[244:247], v[188:191], v[48:51]\n"
"v_exp_f32_e32 v155, v155\n"
"v_mfma_f32_16x16x32_bf16 v[48:51], a[240:243], v[172:175], v[48:51]\n"
"v_cvt_pk_bf16_f32 v153, v154, v155\n"
"v_add_f32_e32 v251, v251, v155\n"
"v_mfma_f32_16x16x32_bf16 v[96:99], a[240:243], v[228:231], v[96:99]\n"
"s_waitcnt lgkmcnt(4)\n"
"v_exp_f32_e32 v156, v156\n"
"v_mfma_f32_16x16x32_bf16 a[160:163], a[248:251], v[160:163], a[160:163]\n"
"s_add_u32 m0, s57, s59\n"
"ds_read_b64_tr_b16 a[240:241], v241 offset:128\n"
"ds_read_b64_tr_b16 a[242:243], v241 offset:6272\n"
"v_mfma_f32_16x16x32_bf16 a[160:163], a[252:255], v[176:179], a[160:163]\n"
"s_waitcnt vmcnt(6)\n"
"buffer_load_dwordx4 v246, s[40:43], s56 offen offset:0 nt lds\n"
"v_mfma_f32_16x16x32_bf16 a[164:167], a[252:255], v[180:183], a[164:167]\n"
"v_exp_f32_e32 v157, v157\n"
"v_mfma_f32_16x16x32_bf16 a[164:167], a[248:251], v[164:167], a[164:167]\n"
"v_add_f32_e32 v252, v252, v156\n"
"v_cvt_pk_bf16_f32 v156, v156, v157\n"
"v_mfma_f32_16x16x32_bf16 v[20:23], a[248:251], v[168:171], v[20:23]\n"
"ds_read_b64_tr_b16 a[244:245], v245 offset:128\n"
"ds_read_b64_tr_b16 a[246:247], v245 offset:6272\n"
"v_mfma_f32_16x16x32_bf16 v[20:23], a[252:255], v[184:187], v[20:23]\n"
"v_exp_f32_e32 v158, v158\n"
"v_mfma_f32_16x16x32_bf16 v[52:55], a[252:255], v[188:191], v[52:55]\n"
"v_add_f32_e32 v252, v252, v157\n"
"v_add_f32_e32 v252, v252, v158\n"
"v_mfma_f32_16x16x32_bf16 v[52:55], a[248:251], v[172:175], v[52:55]\n"
"v_exp_f32_e32 v159, v159\n"
"v_mfma_f32_16x16x32_bf16 v[100:103], a[248:251], v[228:231], v[100:103]\n"
"s_waitcnt lgkmcnt(4)\n"
"v_cvt_pk_bf16_f32 v157, v158, v159\n"
"v_add_f32_e32 v252, v252, v159\n"
"v_mfma_f32_16x16x32_bf16 a[168:171], a[232:235], v[160:163], a[168:171]\n"
"ds_read_b64_tr_b16 a[248:249], v238 offset:256\n"
"ds_read_b64_tr_b16 a[250:251], v238 offset:6400\n"
"v_mfma_f32_16x16x32_bf16 a[168:171], a[236:239], v[176:179], a[168:171]\n"
"v_exp_f32_e32 v208, v208\n"
"v_mfma_f32_16x16x32_bf16 a[172:175], a[236:239], v[180:183], a[172:175]\n"
"v_exp_f32_e32 v209, v209\n"
"v_mfma_f32_16x16x32_bf16 a[172:175], a[232:235], v[164:167], a[172:175]\n"
"v_cvt_pk_bf16_f32 v146, v208, v209\n"
"v_add_f32_e32 v249, v249, v208\n"
"v_mfma_f32_16x16x32_bf16 v[24:27], a[232:235], v[168:171], v[24:27]\n"
"ds_read_b64_tr_b16 a[252:253], v242 offset:256\n"
"ds_read_b64_tr_b16 a[254:255], v242 offset:6400\n"
"v_mfma_f32_16x16x32_bf16 v[24:27], a[236:239], v[184:187], v[24:27]\n"
"v_exp_f32_e32 v210, v210\n"
"v_mfma_f32_16x16x32_bf16 v[56:59], a[236:239], v[188:191], v[56:59]\n"
"s_waitcnt vmcnt(6)\n"
"buffer_load_dwordx4 v247, s[40:43], s56 offen offset:1024 nt lds\n"
"v_mfma_f32_16x16x32_bf16 v[56:59], a[232:235], v[172:175], v[56:59]\n"
"v_add_f32_e32 v249, v249, v209\n"
"v_add_f32_e32 v249, v249, v210\n"
"v_mfma_f32_16x16x32_bf16 v[104:107], a[232:235], v[228:231], v[104:107]\n"
"s_waitcnt lgkmcnt(4)\n"
"v_exp_f32_e32 v211, v211\n"
"v_mfma_f32_16x16x32_bf16 a[176:179], a[240:243], v[160:163], a[176:179]\n"
"ds_read_b64_tr_b16 a[232:233], v239 offset:256\n"
"ds_read_b64_tr_b16 a[234:235], v239 offset:6400\n"
"v_mfma_f32_16x16x32_bf16 a[176:179], a[244:247], v[176:179], a[176:179]\n"
"v_cvt_pk_bf16_f32 v147, v210, v211\n"
"v_add_f32_e32 v249, v249, v211\n"
"v_mfma_f32_16x16x32_bf16 a[180:183], a[244:247], v[180:183], a[180:183]\n"
"v_exp_f32_e32 v212, v212\n"
"v_mfma_f32_16x16x32_bf16 a[180:183], a[240:243], v[164:167], a[180:183]\n"
"v_exp_f32_e32 v213, v213\n"
"v_mfma_f32_16x16x32_bf16 v[28:31], a[240:243], v[168:171], v[28:31]\n"
"ds_read_b64_tr_b16 a[236:237], v243 offset:256\n"
"ds_read_b64_tr_b16 a[238:239], v243 offset:6400\n"
"v_mfma_f32_16x16x32_bf16 v[28:31], a[244:247], v[184:187], v[28:31]\n"
"v_cvt_pk_bf16_f32 v150, v212, v213\n"
"v_add_f32_e32 v250, v250, v212\n"
"v_mfma_f32_16x16x32_bf16 v[60:63], a[244:247], v[188:191], v[60:63]\n"
"v_exp_f32_e32 v214, v214\n"
"v_mfma_f32_16x16x32_bf16 v[60:63], a[240:243], v[172:175], v[60:63]\n"
"v_add_f32_e32 v250, v250, v213\n"
"v_add_f32_e32 v250, v250, v214\n"
"v_mfma_f32_16x16x32_bf16 v[108:111], a[240:243], v[228:231], v[108:111]\n"
"s_waitcnt lgkmcnt(4)\n"
"v_exp_f32_e32 v215, v215\n"
"v_mfma_f32_16x16x32_bf16 a[184:187], a[248:251], v[160:163], a[184:187]\n"
"ds_read_b64_tr_b16 a[240:241], v240 offset:256\n"
"ds_read_b64_tr_b16 a[242:243], v240 offset:6400\n"
"v_mfma_f32_16x16x32_bf16 a[184:187], a[252:255], v[176:179], a[184:187]\n"
"v_cvt_pk_bf16_f32 v151, v214, v215\n"
"v_add_f32_e32 v250, v250, v215\n"
"v_mfma_f32_16x16x32_bf16 a[188:191], a[252:255], v[180:183], a[188:191]\n"
"v_exp_f32_e32 v216, v216\n"
"v_mfma_f32_16x16x32_bf16 a[188:191], a[248:251], v[164:167], a[188:191]\n"
"v_exp_f32_e32 v217, v217\n"
"v_mfma_f32_16x16x32_bf16 a[216:219], a[248:251], v[168:171], a[216:219]\n"
"ds_read_b64_tr_b16 a[244:245], v244 offset:256\n"
"ds_read_b64_tr_b16 a[246:247], v244 offset:6400\n"
"v_mfma_f32_16x16x32_bf16 a[216:219], a[252:255], v[184:187], a[216:219]\n"
"s_waitcnt vmcnt(6)\n"
"buffer_load_dwordx4 v248, s[40:43], s56 offen offset:2048 nt lds\n"
"v_mfma_f32_16x16x32_bf16 v[64:67], a[252:255], v[188:191], v[64:67]\n"
"s_add_u32 s55, s55, 24576\n"
"v_cvt_pk_bf16_f32 v154, v216, v217\n"
"v_add_f32_e32 v251, v251, v216\n"
"v_mfma_f32_16x16x32_bf16 v[64:67], a[248:251], v[172:175], v[64:67]\n"
"s_add_u32 s56, s56, 24576\n"
"v_exp_f32_e32 v218, v218\n"
"v_mfma_f32_16x16x32_bf16 v[112:115], a[248:251], v[228:231], v[112:115]\n"
"s_waitcnt lgkmcnt(4)\n"
"v_add_f32_e32 v251, v251, v217\n"
"v_add_f32_e32 v251, v251, v218\n"
"v_mfma_f32_16x16x32_bf16 a[192:195], a[232:235], v[160:163], a[192:195]\n"
"s_mov_b32 s79, s57\n"
"ds_read_b64_tr_b16 a[248:249], v241 offset:256\n"
"ds_read_b64_tr_b16 a[250:251], v241 offset:6400\n"
"v_mfma_f32_16x16x32_bf16 a[192:195], a[236:239], v[176:179], a[192:195]\n"
"s_add_u32 s57, s57, 24576\n"
"ds_read_b64_tr_b16 a[252:253], v245 offset:256\n"
"ds_read_b64_tr_b16 a[254:255], v245 offset:6400\n"
"v_mfma_f32_16x16x32_bf16 a[196:199], a[236:239], v[180:183], a[196:199]\n"
"s_cmp_eq_u32 s57, 73728\n"
"v_add_u32_e32 v238, s64, v238\n"
"v_add_u32_e32 v239, s64, v239\n"
"v_mfma_f32_16x16x32_bf16 a[196:199], a[232:235], v[164:167], a[196:199]\n"
"s_cselect_b32 s57, 0, s57\n"
"v_add_u32_e32 v240, s64, v240\n"
"v_add_u32_e32 v241, s64, v241\n"
"v_mfma_f32_16x16x32_bf16 a[220:223], a[232:235], v[168:171], a[220:223]\n"
"v_add_u32_e32 v242, s64, v242\n"
"v_add_u32_e32 v243, s64, v243\n"
"v_mfma_f32_16x16x32_bf16 a[220:223], a[236:239], v[184:187], a[220:223]\n"
"v_add_u32_e32 v244, s64, v244\n"
"v_add_u32_e32 v245, s64, v245\n"
"v_mfma_f32_16x16x32_bf16 v[68:71], a[236:239], v[188:191], v[68:71]\n"
"v_exp_f32_e32 v219, v219\n"
"v_mfma_f32_16x16x32_bf16 v[68:71], a[232:235], v[172:175], v[68:71]\n"
"v_cvt_pk_bf16_f32 v155, v218, v219\n"
"v_add_f32_e32 v251, v251, v219\n"
"v_mfma_f32_16x16x32_bf16 v[116:119], a[232:235], v[228:231], v[116:119]\n"
"s_waitcnt vmcnt(12) lgkmcnt(0)\n"
"s_barrier\n"
"v_mfma_f32_16x16x32_bf16 a[200:203], a[240:243], v[160:163], a[200:203]\n"
"ds_read_b128 a[232:235], v236 offset:0\n"
"v_mfma_f32_16x16x32_bf16 a[200:203], a[244:247], v[176:179], a[200:203]\n"
"v_exp_f32_e32 v220, v220\n"
"v_mfma_f32_16x16x32_bf16 a[204:207], a[244:247], v[180:183], a[204:207]\n"
"v_exp_f32_e32 v221, v221\n"
"v_mfma_f32_16x16x32_bf16 a[204:207], a[240:243], v[164:167], a[204:207]\n"
"v_cvt_pk_bf16_f32 v158, v220, v221\n"
"v_add_f32_e32 v252, v252, v220\n"
"v_mfma_f32_16x16x32_bf16 a[224:227], a[240:243], v[168:171], a[224:227]\n"
"ds_read_b128 a[236:239], v237 offset:0\n"
"v_add_f32_e32 v252, v252, v221\n"
"v_mfma_f32_16x16x32_bf16 a[224:227], a[244:247], v[184:187], a[224:227]\n"
"v_exp_f32_e32 v222, v222\n"
"v_mfma_f32_16x16x32_bf16 v[72:75], a[244:247], v[188:191], v[72:75]\n"
"v_exp_f32_e32 v223, v223\n"
"v_mfma_f32_16x16x32_bf16 v[72:75], a[240:243], v[172:175], v[72:75]\n"
"v_cvt_pk_bf16_f32 v159, v222, v223\n"
"v_add_f32_e32 v252, v252, v222\n"
"v_mfma_f32_16x16x32_bf16 v[120:123], a[240:243], v[228:231], v[120:123]\n"
"v_add_f32_e32 v252, v252, v223\n"
"v_mfma_f32_16x16x32_bf16 a[208:211], a[248:251], v[160:163], a[208:211]\n"
"ds_read_b128 a[240:243], v236 offset:128\n"
"v_mfma_f32_16x16x32_bf16 a[208:211], a[252:255], v[176:179], a[208:211]\n"
"v_mfma_f32_16x16x32_bf16 a[212:215], a[252:255], v[180:183], a[212:215]\n"
"v_mfma_f32_16x16x32_bf16 a[212:215], a[248:251], v[164:167], a[212:215]\n"
"global_load_dword v255, v254, s[74:75]\n"
"v_mfma_f32_16x16x32_bf16 a[228:231], a[248:251], v[168:171], a[228:231]\n"
"ds_read_b128 a[244:247], v237 offset:128\n"
"v_mfma_f32_16x16x32_bf16 a[228:231], a[252:255], v[184:187], a[228:231]\n"
"v_mfma_f32_16x16x32_bf16 v[76:79], a[252:255], v[188:191], v[76:79]\n"
"v_mfma_f32_16x16x32_bf16 v[76:79], a[248:251], v[172:175], v[76:79]\n"
"v_mfma_f32_16x16x32_bf16 v[124:127], a[248:251], v[228:231], v[124:127]\n"
"s_waitcnt lgkmcnt(2)\n"
"v_mfma_f32_16x16x32_bf16 v[160:163], a[232:235], a[0:3], 0\n"
"ds_read_b128 a[248:251], v236 offset:256\n"
"v_mfma_f32_16x16x32_bf16 v[160:163], a[236:239], a[4:7], v[160:163]\n"
"s_cmp_eq_u32 s57, 24576\n"
"v_mfma_f32_16x16x32_bf16 v[164:167], a[236:239], a[28:31], 0\n"
"s_cselect_b32 s63, s66, s65\n"
"v_mfma_f32_16x16x32_bf16 v[164:167], a[232:235], a[24:27], v[164:167]\n"
"s_cselect_b32 s64, s77, s76\n"
"v_mfma_f32_16x16x32_bf16 v[168:171], a[232:235], a[48:51], 0\n"
"v_mfma_f32_16x16x32_bf16 v[168:171], a[236:239], a[52:55], v[168:171]\n"
"ds_read_b128 a[252:255], v237 offset:256\n"
"v_mfma_f32_16x16x32_bf16 v[172:175], a[236:239], a[76:79], 0\n"
"v_mfma_f32_16x16x32_bf16 v[172:175], a[232:235], a[72:75], v[172:175]\n"
"s_add_u32 m0, s79, s60\n"
"v_mfma_f32_16x16x32_bf16 v[228:231], a[232:235], a[96:99], 0\n"
"s_waitcnt vmcnt(6)\n"
"buffer_load_dwordx4 v246, s[44:47], s55 offen offset:0 nt lds\n"
"v_mfma_f32_16x16x32_bf16 v[228:231], a[236:239], a[100:103], v[228:231]\n"
"s_waitcnt lgkmcnt(2)\n"
"v_mfma_f32_16x16x32_bf16 v[160:163], a[240:243], a[8:11], v[160:163]\n"
"ds_read_b128 a[232:235], v236 offset:6144\n"
"v_mfma_f32_16x16x32_bf16 v[160:163], a[244:247], a[12:15], v[160:163]\n"
"v_mfma_f32_16x16x32_bf16 v[164:167], a[244:247], a[36:39], v[164:167]\n"
"v_mfma_f32_16x16x32_bf16 v[164:167], a[240:243], a[32:35], v[164:167]\n"
"v_mfma_f32_16x16x32_bf16 v[168:171], a[240:243], a[56:59], v[168:171]\n"
"v_mfma_f32_16x16x32_bf16 v[168:171], a[244:247], a[60:63], v[168:171]\n"
"ds_read_b128 a[236:239], v237 offset:6144\n"
"v_mfma_f32_16x16x32_bf16 v[172:175], a[244:247], a[84:87], v[172:175]\n"
"v_mfma_f32_16x16x32_bf16 v[172:175], a[240:243], a[80:83], v[172:175]\n"
"v_mfma_f32_16x16x32_bf16 v[228:231], a[240:243], a[104:107], v[228:231]\n"
"v_mfma_f32_16x16x32_bf16 v[228:231], a[244:247], a[108:111], v[228:231]\n"
"s_waitcnt lgkmcnt(2)\n"
"v_mfma_f32_16x16x32_bf16 v[160:163], a[248:251], a[16:19], v[160:163]\n"
"ds_read_b128 a[240:243], v236 offset:6272\n"
"v_mfma_f32_16x16x32_bf16 v[160:163], a[252:255], a[20:23], v[160:163]\n"
"v_mfma_f32_16x16x32_bf16 v[164:167], a[252:255], a[44:47], v[164:167]\n"
"v_mfma_f32_16x16x32_bf16 v[164:167], a[248:251], a[40:43], v[164:167]\n"
"v_mfma_f32_16x16x32_bf16 v[168:171], a[248:251], a[64:67], v[168:171]\n"
"s_waitcnt vmcnt(6)\n"
"buffer_load_dwordx4 v247, s[44:47], s55 offen offset:1024 nt lds\n"
"v_mfma_f32_16x16x32_bf16 v[168:171], a[252:255], a[68:71], v[168:171]\n"
"ds_read_b128 a[244:247], v237 offset:6272\n"
"v_mfma_f32_16x16x32_bf16 v[172:175], a[252:255], a[92:95], v[172:175]\n"
"v_mfma_f32_16x16x32_bf16 v[172:175], a[248:251], a[88:91], v[172:175]\n"
"v_mfma_f32_16x16x32_bf16 v[228:231], a[248:251], a[112:115], v[228:231]\n"
"v_mfma_f32_16x16x32_bf16 v[228:231], a[252:255], a[116:119], v[228:231]\n"
"s_waitcnt lgkmcnt(2)\n"
"v_exp_f32_e32 v160, v160\n"
"v_mfma_f32_16x16x32_bf16 v[192:195], a[232:235], a[0:3], 0\n"
"ds_read_b128 a[248:251], v236 offset:6400\n"
"v_add_f32_e32 v249, v249, v160\n"
"v_mfma_f32_16x16x32_bf16 v[192:195], a[236:239], a[4:7], v[192:195]\n"
"v_add_u32_e32 v236, s62, v236\n"
"v_mfma_f32_16x16x32_bf16 v[196:199], a[236:239], a[28:31], 0\n"
"v_exp_f32_e32 v161, v161\n"
"v_mfma_f32_16x16x32_bf16 v[196:199], a[232:235], a[24:27], v[196:199]\n"
"v_cvt_pk_bf16_f32 v160, v160, v161\n"
"v_add_f32_e32 v249, v249, v161\n"
"v_mfma_f32_16x16x32_bf16 v[200:203], a[232:235], a[48:51], 0\n"
"v_exp_f32_e32 v162, v162\n"
"v_mfma_f32_16x16x32_bf16 v[200:203], a[236:239], a[52:55], v[200:203]\n"
"ds_read_b128 a[252:255], v237 offset:6400\n"
"v_add_f32_e32 v249, v249, v162\n"
"v_mfma_f32_16x16x32_bf16 v[204:207], a[236:239], a[76:79], 0\n"
"v_add_u32_e32 v237, s62, v237\n"
"v_mfma_f32_16x16x32_bf16 v[204:207], a[232:235], a[72:75], v[204:207]\n"
"v_exp_f32_e32 v163, v163\n"
"v_mfma_f32_16x16x32_bf16 v[232:235], a[232:235], a[96:99], 0\n"
"v_cvt_pk_bf16_f32 v161, v162, v163\n"
"v_add_f32_e32 v249, v249, v163\n"
"v_mfma_f32_16x16x32_bf16 v[232:235], a[236:239], a[100:103], v[232:235]\n"
"s_waitcnt lgkmcnt(2)\n"
"v_exp_f32_e32 v164, v164\n"
"v_mfma_f32_16x16x32_bf16 v[192:195], a[240:243], a[8:11], v[192:195]\n"
"ds_read_b128 a[232:235], v236 offset:0\n"
"v_add_f32_e32 v250, v250, v164\n"
"v_mfma_f32_16x16x32_bf16 v[192:195], a[244:247], a[12:15], v[192:195]\n"
"s_waitcnt vmcnt(6)\n"
"buffer_load_dwordx4 v248, s[44:47], s55 offen offset:2048 nt lds\n"
"v_mfma_f32_16x16x32_bf16 v[196:199], a[244:247], a[36:39], v[196:199]\n"
"v_exp_f32_e32 v165, v165\n"
"v_mfma_f32_16x16x32_bf16 v[196:199], a[240:243], a[32:35], v[196:199]\n"
"v_cvt_pk_bf16_f32 v164, v164, v165\n"
"v_add_f32_e32 v250, v250, v165\n"
"v_mfma_f32_16x16x32_bf16 v[200:203], a[240:243], a[56:59], v[200:203]\n"
"v_exp_f32_e32 v166, v166\n"
"v_mfma_f32_16x16x32_bf16 v[200:203], a[244:247], a[60:63], v[200:203]\n"
"ds_read_b128 a[236:239], v237 offset:0\n"
"v_add_f32_e32 v250, v250, v166\n"
"v_mfma_f32_16x16x32_bf16 v[204:207], a[244:247], a[84:87], v[204:207]\n"
"v_exp_f32_e32 v167, v167\n"
"v_mfma_f32_16x16x32_bf16 v[204:207], a[240:243], a[80:83], v[204:207]\n"
"v_cvt_pk_bf16_f32 v165, v166, v167\n"
"v_add_f32_e32 v250, v250, v167\n"
"v_mfma_f32_16x16x32_bf16 v[232:235], a[240:243], a[104:107], v[232:235]\n"
"v_exp_f32_e32 v168, v168\n"
"v_mfma_f32_16x16x32_bf16 v[232:235], a[244:247], a[108:111], v[232:235]\n"
"s_waitcnt lgkmcnt(2)\n"
"v_exp_f32_e32 v169, v169\n"
"v_mfma_f32_16x16x32_bf16 v[192:195], a[248:251], a[16:19], v[192:195]\n"
"ds_read_b128 a[240:243], v236 offset:128\n"
"v_add_f32_e32 v251, v251, v168\n"
"v_mfma_f32_16x16x32_bf16 v[192:195], a[252:255], a[20:23], v[192:195]\n"
"v_cvt_pk_bf16_f32 v168, v168, v169\n"
"v_add_f32_e32 v251, v251, v169\n"
"v_mfma_f32_16x16x32_bf16 v[196:199], a[252:255], a[44:47], v[196:199]\n"
"v_exp_f32_e32 v170, v170\n"
"v_mfma_f32_16x16x32_bf16 v[196:199], a[248:251], a[40:43], v[196:199]\n"
"v_exp_f32_e32 v171, v171\n"
"v_mfma_f32_16x16x32_bf16 v[200:203], a[248:251], a[64:67], v[200:203]\n"
"v_cvt_pk_bf16_f32 v169, v170, v171\n"
"v_add_f32_e32 v251, v251, v170\n"
"v_mfma_f32_16x16x32_bf16 v[200:203], a[252:255], a[68:71], v[200:203]\n"
"s_add_u32 m0, s79, s61\n"
"ds_read_b128 a[244:247], v237 offset:128\n"
"v_add_f32_e32 v251, v251, v171\n"
"v_mfma_f32_16x16x32_bf16 v[204:207], a[252:255], a[92:95], v[204:207]\n"
"s_waitcnt vmcnt(6)\n"
"buffer_load_dwordx4 v246, s[44:47], s56 offen offset:0 nt lds\n"
"v_mfma_f32_16x16x32_bf16 v[204:207], a[248:251], a[88:91], v[204:207]\n"
"v_exp_f32_e32 v172, v172\n"
"v_mfma_f32_16x16x32_bf16 v[232:235], a[248:251], a[112:115], v[232:235]\n"
"v_exp_f32_e32 v173, v173\n"
"v_mfma_f32_16x16x32_bf16 v[232:235], a[252:255], a[116:119], v[232:235]\n"
"s_waitcnt lgkmcnt(2)\n"
"v_add_f32_e32 v252, v252, v172\n"
"v_cvt_pk_bf16_f32 v172, v172, v173\n"
"v_mfma_f32_16x16x32_bf16 v[176:179], a[232:235], a[0:3], 0\n"
"ds_read_b128 a[248:251], v236 offset:256\n"
"v_add_f32_e32 v252, v252, v173\n"
"v_mfma_f32_16x16x32_bf16 v[176:179], a[236:239], a[4:7], v[176:179]\n"
"v_exp_f32_e32 v174, v174\n"
"v_mfma_f32_16x16x32_bf16 v[180:183], a[236:239], a[28:31], 0\n"
"v_exp_f32_e32 v175, v175\n"
"v_mfma_f32_16x16x32_bf16 v[180:183], a[232:235], a[24:27], v[180:183]\n"
"v_cvt_pk_bf16_f32 v173, v174, v175\n"
"v_add_f32_e32 v252, v252, v174\n"
"v_mfma_f32_16x16x32_bf16 v[184:187], a[232:235], a[48:51], 0\n"
"ds_read_b128 a[252:255], v237 offset:256\n"
"v_add_f32_e32 v252, v252, v175\n"
"v_mfma_f32_16x16x32_bf16 v[184:187], a[236:239], a[52:55], v[184:187]\n"
"v_exp_f32_e32 v228, v228\n"
"v_mfma_f32_16x16x32_bf16 v[188:191], a[236:239], a[76:79], 0\n"
"v_exp_f32_e32 v229, v229\n"
"v_mfma_f32_16x16x32_bf16 v[188:191], a[232:235], a[72:75], v[188:191]\n"
"s_waitcnt lgkmcnt(2)\n"
"v_add_f32_e32 v253, v253, v228\n"
"v_cvt_pk_bf16_f32 v228, v228, v229\n"
"v_mfma_f32_16x16x32_bf16 v[176:179], a[240:243], a[8:11], v[176:179]\n"
"ds_read_b128 a[232:235], v236 offset:6144\n"
"v_add_f32_e32 v253, v253, v229\n"
"v_mfma_f32_16x16x32_bf16 v[176:179], a[244:247], a[12:15], v[176:179]\n"
"v_exp_f32_e32 v230, v230\n"
"v_mfma_f32_16x16x32_bf16 v[180:183], a[244:247], a[36:39], v[180:183]\n"
"v_exp_f32_e32 v231, v231\n"
"v_mfma_f32_16x16x32_bf16 v[180:183], a[240:243], a[32:35], v[180:183]\n"
"v_cvt_pk_bf16_f32 v229, v230, v231\n"
"v_add_f32_e32 v253, v253, v230\n"
"v_mfma_f32_16x16x32_bf16 v[184:187], a[240:243], a[56:59], v[184:187]\n"
"ds_read_b128 a[236:239], v237 offset:6144\n"
"v_add_f32_e32 v253, v253, v231\n"
"v_mfma_f32_16x16x32_bf16 v[184:187], a[244:247], a[60:63], v[184:187]\n"
"s_waitcnt vmcnt(6)\n"
"buffer_load_dwordx4 v247, s[44:47], s56 offen offset:1024 nt lds\n"
"v_mfma_f32_16x16x32_bf16 v[188:191], a[244:247], a[84:87], v[188:191]\n"
"v_exp_f32_e32 v192, v192\n"
"v_mfma_f32_16x16x32_bf16 v[188:191], a[240:243], a[80:83], v[188:191]\n"
"s_waitcnt lgkmcnt(2)\n"
"v_exp_f32_e32 v193, v193\n"
"v_mfma_f32_16x16x32_bf16 v[176:179], a[248:251], a[16:19], v[176:179]\n"
"ds_read_b128 a[240:243], v236 offset:6272\n"
"v_cvt_pk_bf16_f32 v162, v192, v193\n"
"v_mfma_f32_16x16x32_bf16 v[176:179], a[252:255], a[20:23], v[176:179]\n"
"v_add_f32_e32 v249, v249, v192\n"
"v_add_f32_e32 v249, v249, v193\n"
"v_mfma_f32_16x16x32_bf16 v[180:183], a[252:255], a[44:47], v[180:183]\n"
"v_exp_f32_e32 v194, v194\n"
"v_mfma_f32_16x16x32_bf16 v[180:183], a[248:251], a[40:43], v[180:183]\n"
"v_exp_f32_e32 v195, v195\n"
"v_mfma_f32_16x16x32_bf16 v[184:187], a[248:251], a[64:67], v[184:187]\n"
"ds_read_b128 a[244:247], v237 offset:6272\n"
"v_cvt_pk_bf16_f32 v163, v194, v195\n"
"v_mfma_f32_16x16x32_bf16 v[184:187], a[252:255], a[68:71], v[184:187]\n"
"v_add_f32_e32 v249, v249, v194\n"
"v_add_f32_e32 v249, v249, v195\n"
"v_mfma_f32_16x16x32_bf16 v[188:191], a[252:255], a[92:95], v[188:191]\n"
"v_exp_f32_e32 v196, v196\n"
"v_mfma_f32_16x16x32_bf16 v[188:191], a[248:251], a[88:91], v[188:191]\n"
"s_waitcnt lgkmcnt(2)\n"
"v_exp_f32_e32 v197, v197\n"
"v_mfma_f32_16x16x32_bf16 v[208:211], a[232:235], a[0:3], 0\n"
"ds_read_b128 a[248:251], v236 offset:6400\n"
"v_cvt_pk_bf16_f32 v166, v196, v197\n"
"v_mfma_f32_16x16x32_bf16 v[208:211], a[236:239], a[4:7], v[208:211]\n"
"v_add_f32_e32 v250, v250, v196\n"
"v_add_f32_e32 v250, v250, v197\n"
"v_mfma_f32_16x16x32_bf16 v[212:215], a[236:239], a[28:31], 0\n"
"v_exp_f32_e32 v198, v198\n"
"v_mfma_f32_16x16x32_bf16 v[212:215], a[232:235], a[24:27], v[212:215]\n"
"v_exp_f32_e32 v199, v199\n"
"v_mfma_f32_16x16x32_bf16 v[216:219], a[232:235], a[48:51], 0\n"
"ds_read_b128 a[252:255], v237 offset:6400\n"
"v_cvt_pk_bf16_f32 v167, v198, v199\n"
"v_mfma_f32_16x16x32_bf16 v[216:219], a[236:239], a[52:55], v[216:219]\n"
"s_waitcnt vmcnt(6)\n"
"buffer_load_dwordx4 v248, s[44:47], s56 offen offset:2048 nt lds\n"
"v_mfma_f32_16x16x32_bf16 v[220:223], a[236:239], a[76:79], 0\n"
"v_add_u32_e32 v236, s63, v236\n"
"v_add_u32_e32 v237, s63, v237\n"
"v_mfma_f32_16x16x32_bf16 v[220:223], a[232:235], a[72:75], v[220:223]\n"
"s_waitcnt vmcnt(12)\n"
"s_barrier\n"
"s_waitcnt lgkmcnt(2)\n"
"v_mfma_f32_16x16x32_bf16 v[208:211], a[240:243], a[8:11], v[208:211]\n"
"ds_read_b64_tr_b16 a[232:233], v238 offset:0\n"
"ds_read_b64_tr_b16 a[234:235], v238 offset:6144\n"
"v_mfma_f32_16x16x32_bf16 v[208:211], a[244:247], a[12:15], v[208:211]\n"
"v_add_f32_e32 v250, v250, v198\n"
"v_add_f32_e32 v250, v250, v199\n"
"v_mfma_f32_16x16x32_bf16 v[212:215], a[244:247], a[36:39], v[212:215]\n"
"v_exp_f32_e32 v200, v200\n"
"v_mfma_f32_16x16x32_bf16 v[212:215], a[240:243], a[32:35], v[212:215]\n"
"v_exp_f32_e32 v201, v201\n"
"v_mfma_f32_16x16x32_bf16 v[216:219], a[240:243], a[56:59], v[216:219]\n"
"ds_read_b64_tr_b16 a[236:237], v242 offset:0\n"
"ds_read_b64_tr_b16 a[238:239], v242 offset:6144\n"
"v_mfma_f32_16x16x32_bf16 v[216:219], a[244:247], a[60:63], v[216:219]\n"
"v_cvt_pk_bf16_f32 v170, v200, v201\n"
"v_add_f32_e32 v251, v251, v200\n"
"v_mfma_f32_16x16x32_bf16 v[220:223], a[244:247], a[84:87], v[220:223]\n"
"v_exp_f32_e32 v202, v202\n"
"v_mfma_f32_16x16x32_bf16 v[220:223], a[240:243], a[80:83], v[220:223]\n"
"s_waitcnt lgkmcnt(4)\n"
"v_add_f32_e32 v251, v251, v201\n"
"v_add_f32_e32 v251, v251, v202\n"
"v_mfma_f32_16x16x32_bf16 v[208:211], a[248:251], a[16:19], v[208:211]\n"
"ds_read_b64_tr_b16 a[240:241], v239 offset:0\n"
"ds_read_b64_tr_b16 a[242:243], v239 offset:6144\n"
"v_mfma_f32_16x16x32_bf16 v[208:211], a[252:255], a[20:23], v[208:211]\n"
"v_exp_f32_e32 v203, v203\n"
"v_mfma_f32_16x16x32_bf16 v[212:215], a[252:255], a[44:47], v[212:215]\n"
"v_cvt_pk_bf16_f32 v171, v202, v203\n"
"v_add_f32_e32 v251, v251, v203\n"
"v_mfma_f32_16x16x32_bf16 v[212:215], a[248:251], a[40:43], v[212:215]\n"
"v_exp_f32_e32 v204, v204\n"
"v_mfma_f32_16x16x32_bf16 v[216:219], a[248:251], a[64:67], v[216:219]\n"
"s_add_u32 m0, s57, s58\n"
"ds_read_b64_tr_b16 a[244:245], v243 offset:0\n"
"ds_read_b64_tr_b16 a[246:247], v243 offset:6144\n"
"v_mfma_f32_16x16x32_bf16 v[216:219], a[252:255], a[68:71], v[216:219]\n"
"s_waitcnt vmcnt(6)\n"
"buffer_load_dwordx4 v246, s[40:43], s55 offen offset:0 nt lds\n"
"v_mfma_f32_16x16x32_bf16 v[220:223], a[252:255], a[92:95], v[220:223]\n"
"v_exp_f32_e32 v205, v205\n"
"v_mfma_f32_16x16x32_bf16 v[220:223], a[248:251], a[88:91], v[220:223]\n"
"s_waitcnt lgkmcnt(4)\n"
"v_cvt_pk_bf16_f32 v174, v204, v205\n"
"v_add_f32_e32 v252, v252, v204\n"
"v_mfma_f32_16x16x32_bf16 a[120:123], a[232:235], v[128:131], a[120:123]\n"
"ds_read_b64_tr_b16 a[248:249], v240 offset:0\n"
"ds_read_b64_tr_b16 a[250:251], v240 offset:6144\n"
"v_mfma_f32_16x16x32_bf16 a[120:123], a[236:239], v[144:147], a[120:123]\n"
"v_exp_f32_e32 v206, v206\n"
"v_mfma_f32_16x16x32_bf16 a[124:127], a[236:239], v[148:151], a[124:127]\n"
"v_add_f32_e32 v252, v252, v205\n"
"v_add_f32_e32 v252, v252, v206\n"
"v_mfma_f32_16x16x32_bf16 a[124:127], a[232:235], v[132:135], a[124:127]\n"
"v_exp_f32_e32 v207, v207\n"
"v_mfma_f32_16x16x32_bf16 v[0:3], a[232:235], v[136:139], v[0:3]\n"
"ds_read_b64_tr_b16 a[252:253], v244 offset:0\n"
"ds_read_b64_tr_b16 a[254:255], v244 offset:6144\n"
"v_mfma_f32_16x16x32_bf16 v[0:3], a[236:239], v[152:155], v[0:3]\n"
"v_cvt_pk_bf16_f32 v175, v206, v207\n"
"v_add_f32_e32 v252, v252, v207\n"
"v_mfma_f32_16x16x32_bf16 v[32:35], a[236:239], v[156:159], v[32:35]\n"
"v_exp_f32_e32 v232, v232\n"
"v_mfma_f32_16x16x32_bf16 v[32:35], a[232:235], v[140:143], v[32:35]\n"
"v_exp_f32_e32 v233, v233\n"
"v_mfma_f32_16x16x32_bf16 v[80:83], a[232:235], v[224:227], v[80:83]\n"
"s_waitcnt lgkmcnt(4)\n"
"v_cvt_pk_bf16_f32 v230, v232, v233\n"
"v_add_f32_e32 v253, v253, v232\n"
"v_mfma_f32_16x16x32_bf16 a[128:131], a[240:243], v[128:131], a[128:131]\n"
"ds_read_b64_tr_b16 a[232:233], v241 offset:0\n"
"ds_read_b64_tr_b16 a[234:235], v241 offset:6144\n"
"v_mfma_f32_16x16x32_bf16 a[128:131], a[244:247], v[144:147], a[128:131]\n"
"v_exp_f32_e32 v234, v234\n"
"v_mfma_f32_16x16x32_bf16 a[132:135], a[244:247], v[148:151], a[132:135]\n"
"v_add_f32_e32 v253, v253, v233\n"
"v_add_f32_e32 v253, v253, v234\n"
"v_mfma_f32_16x16x32_bf16 a[132:135], a[240:243], v[132:135], a[132:135]\n"
"s_waitcnt vmcnt(6)\n"
"buffer_load_dwordx4 v247, s[40:43], s55 offen offset:1024 nt lds\n"
"v_mfma_f32_16x16x32_bf16 v[4:7], a[240:243], v[136:139], v[4:7]\n"
"ds_read_b64_tr_b16 a[236:237], v245 offset:0\n"
"ds_read_b64_tr_b16 a[238:239], v245 offset:6144\n"
"v_mfma_f32_16x16x32_bf16 v[4:7], a[244:247], v[152:155], v[4:7]\n"
"v_exp_f32_e32 v235, v235\n"
"v_mfma_f32_16x16x32_bf16 v[36:39], a[244:247], v[156:159], v[36:39]\n"
"v_cvt_pk_bf16_f32 v231, v234, v235\n"
"v_add_f32_e32 v253, v253, v235\n"
"v_mfma_f32_16x16x32_bf16 v[36:39], a[240:243], v[140:143], v[36:39]\n"
"v_exp_f32_e32 v176, v176\n"
"v_mfma_f32_16x16x32_bf16 v[84:87], a[240:243], v[224:227], v[84:87]\n"
"s_waitcnt lgkmcnt(4)\n"
"v_exp_f32_e32 v177, v177\n"
"v_mfma_f32_16x16x32_bf16 a[136:139], a[248:251], v[128:131], a[136:139]\n"
"ds_read_b64_tr_b16 a[240:241], v238 offset:128\n"
"ds_read_b64_tr_b16 a[242:243], v238 offset:6272\n"
"v_mfma_f32_16x16x32_bf16 a[136:139], a[252:255], v[144:147], a[136:139]\n"
"v_add_f32_e32 v249, v249, v176\n"
"v_cvt_pk_bf16_f32 v176, v176, v177\n"
"v_mfma_f32_16x16x32_bf16 a[140:143], a[252:255], v[148:151], a[140:143]\n"
"v_exp_f32_e32 v178, v178\n"
"v_mfma_f32_16x16x32_bf16 a[140:143], a[248:251], v[132:135], a[140:143]\n"
"v_add_f32_e32 v249, v249, v177\n"
"v_add_f32_e32 v249, v249, v178\n"
"v_mfma_f32_16x16x32_bf16 v[8:11], a[248:251], v[136:139], v[8:11]\n"
"ds_read_b64_tr_b16 a[244:245], v242 offset:128\n"
"ds_read_b64_tr_b16 a[246:247], v242 offset:6272\n"
"v_mfma_f32_16x16x32_bf16 v[8:11], a[252:255], v[152:155], v[8:11]\n"
"v_exp_f32_e32 v179, v179\n"
"v_mfma_f32_16x16x32_bf16 v[40:43], a[252:255], v[156:159], v[40:43]\n"
"v_cvt_pk_bf16_f32 v177, v178, v179\n"
"v_add_f32_e32 v249, v249, v179\n"
"v_mfma_f32_16x16x32_bf16 v[40:43], a[248:251], v[140:143], v[40:43]\n"
"v_exp_f32_e32 v180, v180\n"
"v_mfma_f32_16x16x32_bf16 v[88:91], a[248:251], v[224:227], v[88:91]\n"
"s_waitcnt lgkmcnt(4)\n"
"v_exp_f32_e32 v181, v181\n"
"v_mfma_f32_16x16x32_bf16 a[144:147], a[232:235], v[128:131], a[144:147]\n"
"ds_read_b64_tr_b16 a[248:249], v239 offset:128\n"
"ds_read_b64_tr_b16 a[250:251], v239 offset:6272\n"
"v_mfma_f32_16x16x32_bf16 a[144:147], a[236:239], v[144:147], a[144:147]\n"
"s_waitcnt vmcnt(6)\n"
"buffer_load_dwordx4 v248, s[40:43], s55 offen offset:2048 nt lds\n"
"v_mfma_f32_16x16x32_bf16 a[148:151], a[236:239], v[148:151], a[148:151]\n"
"v_add_f32_e32 v250, v250, v180\n"
"v_cvt_pk_bf16_f32 v180, v180, v181\n"
"v_mfma_f32_16x16x32_bf16 a[148:151], a[232:235], v[132:135], a[148:151]\n"
"v_exp_f32_e32 v182, v182\n"
"v_mfma_f32_16x16x32_bf16 v[12:15], a[232:235], v[136:139], v[12:15]\n"
"ds_read_b64_tr_b16 a[252:253], v243 offset:128\n"
"ds_read_b64_tr_b16 a[254:255], v243 offset:6272\n"
"v_mfma_f32_16x16x32_bf16 v[12:15], a[236:239], v[152:155], v[12:15]\n"
"v_add_f32_e32 v250, v250, v181\n"
"v_add_f32_e32 v250, v250, v182\n"
"v_mfma_f32_16x16x32_bf16 v[44:47], a[236:239], v[156:159], v[44:47]\n"
"v_exp_f32_e32 v183, v183\n"
"v_mfma_f32_16x16x32_bf16 v[44:47], a[232:235], v[140:143], v[44:47]\n"
"v_cvt_pk_bf16_f32 v181, v182, v183\n"
"v_add_f32_e32 v250, v250, v183\n"
"v_mfma_f32_16x16x32_bf16 v[92:95], a[232:235], v[224:227], v[92:95]\n"
"s_waitcnt lgkmcnt(4)\n"
"v_exp_f32_e32 v184, v184\n"
"v_mfma_f32_16x16x32_bf16 a[152:155], a[240:243], v[128:131], a[152:155]\n"
"ds_read_b64_tr_b16 a[232:233], v240 offset:128\n"
"ds_read_b64_tr_b16 a[234:235], v240 offset:6272\n"
"v_mfma_f32_16x16x32_bf16 a[152:155], a[244:247], v[144:147], a[152:155]\n"
"v_exp_f32_e32 v185, v185\n"
"v_mfma_f32_16x16x32_bf16 a[156:159], a[244:247], v[148:151], a[156:159]\n"
"v_add_f32_e32 v251, v251, v184\n"
"v_cvt_pk_bf16_f32 v184, v184, v185\n"
"v_mfma_f32_16x16x32_bf16 a[156:159], a[240:243], v[132:135], a[156:159]\n"
"v_exp_f32_e32 v186, v186\n"
"v_mfma_f32_16x16x32_bf16 v[16:19], a[240:243], v[136:139], v[16:19]\n"
"ds_read_b64_tr_b16 a[236:237], v244 offset:128\n"
"ds_read_b64_tr_b16 a[238:239], v244 offset:6272\n"
"v_mfma_f32_16x16x32_bf16 v[16:19], a[244:247], v[152:155], v[16:19]\n"
"v_add_f32_e32 v251, v251, v185\n"
"v_add_f32_e32 v251, v251, v186\n"
"v_mfma_f32_16x16x32_bf16 v[48:51], a[244:247], v[156:159], v[48:51]\n"
"v_exp_f32_e32 v187, v187\n"
"v_mfma_f32_16x16x32_bf16 v[48:51], a[240:243], v[140:143], v[48:51]\n"
"v_cvt_pk_bf16_f32 v185, v186, v187\n"
"v_add_f32_e32 v251, v251, v187\n"
"v_mfma_f32_16x16x32_bf16 v[96:99], a[240:243], v[224:227], v[96:99]\n"
"s_waitcnt lgkmcnt(4)\n"
"v_exp_f32_e32 v188, v188\n"
"v_mfma_f32_16x16x32_bf16 a[160:163], a[248:251], v[128:131], a[160:163]\n"
"s_add_u32 m0, s57, s59\n"
"ds_read_b64_tr_b16 a[240:241], v241 offset:128\n"
"ds_read_b64_tr_b16 a[242:243], v241 offset:6272\n"
"v_mfma_f32_16x16x32_bf16 a[160:163], a[252:255], v[144:147], a[160:163]\n"
"s_waitcnt vmcnt(6)\n"
"buffer_load_dwordx4 v246, s[40:43], s56 offen offset:0 nt lds\n"
"v_mfma_f32_16x16x32_bf16 a[164:167], a[252:255], v[148:151], a[164:167]\n"
"v_exp_f32_e32 v189, v189\n"
"v_mfma_f32_16x16x32_bf16 a[164:167], a[248:251], v[132:135], a[164:167]\n"
"v_add_f32_e32 v252, v252, v188\n"
"v_cvt_pk_bf16_f32 v188, v188, v189\n"
"v_mfma_f32_16x16x32_bf16 v[20:23], a[248:251], v[136:139], v[20:23]\n"
"ds_read_b64_tr_b16 a[244:245], v245 offset:128\n"
"ds_read_b64_tr_b16 a[246:247], v245 offset:6272\n"
"v_mfma_f32_16x16x32_bf16 v[20:23], a[252:255], v[152:155], v[20:23]\n"
"v_exp_f32_e32 v190, v190\n"
"v_mfma_f32_16x16x32_bf16 v[52:55], a[252:255], v[156:159], v[52:55]\n"
"v_add_f32_e32 v252, v252, v189\n"
"v_add_f32_e32 v252, v252, v190\n"
"v_mfma_f32_16x16x32_bf16 v[52:55], a[248:251], v[140:143], v[52:55]\n"
"v_exp_f32_e32 v191, v191\n"
"v_mfma_f32_16x16x32_bf16 v[100:103], a[248:251], v[224:227], v[100:103]\n"
"s_waitcnt lgkmcnt(4)\n"
"v_cvt_pk_bf16_f32 v189, v190, v191\n"
"v_add_f32_e32 v252, v252, v191\n"
"v_mfma_f32_16x16x32_bf16 a[168:171], a[232:235], v[128:131], a[168:171]\n"
"ds_read_b64_tr_b16 a[248:249], v238 offset:256\n"
"ds_read_b64_tr_b16 a[250:251], v238 offset:6400\n"
"v_mfma_f32_16x16x32_bf16 a[168:171], a[236:239], v[144:147], a[168:171]\n"
"v_exp_f32_e32 v208, v208\n"
"v_mfma_f32_16x16x32_bf16 a[172:175], a[236:239], v[148:151], a[172:175]\n"
"v_exp_f32_e32 v209, v209\n"
"v_mfma_f32_16x16x32_bf16 a[172:175], a[232:235], v[132:135], a[172:175]\n"
"v_cvt_pk_bf16_f32 v178, v208, v209\n"
"v_add_f32_e32 v249, v249, v208\n"
"v_mfma_f32_16x16x32_bf16 v[24:27], a[232:235], v[136:139], v[24:27]\n"
"ds_read_b64_tr_b16 a[252:253], v242 offset:256\n"
"ds_read_b64_tr_b16 a[254:255], v242 offset:6400\n"
"v_mfma_f32_16x16x32_bf16 v[24:27], a[236:239], v[152:155], v[24:27]\n"
"v_exp_f32_e32 v210, v210\n"
"v_mfma_f32_16x16x32_bf16 v[56:59], a[236:239], v[156:159], v[56:59]\n"
"s_waitcnt vmcnt(6)\n"
"buffer_load_dwordx4 v247, s[40:43], s56 offen offset:1024 nt lds\n"
"v_mfma_f32_16x16x32_bf16 v[56:59], a[232:235], v[140:143], v[56:59]\n"
"v_add_f32_e32 v249, v249, v209\n"
"v_add_f32_e32 v249, v249, v210\n"
"v_mfma_f32_16x16x32_bf16 v[104:107], a[232:235], v[224:227], v[104:107]\n"
"s_waitcnt lgkmcnt(4)\n"
"v_exp_f32_e32 v211, v211\n"
"v_mfma_f32_16x16x32_bf16 a[176:179], a[240:243], v[128:131], a[176:179]\n"
"ds_read_b64_tr_b16 a[232:233], v239 offset:256\n"
"ds_read_b64_tr_b16 a[234:235], v239 offset:6400\n"
"v_mfma_f32_16x16x32_bf16 a[176:179], a[244:247], v[144:147], a[176:179]\n"
"v_cvt_pk_bf16_f32 v179, v210, v211\n"
"v_add_f32_e32 v249, v249, v211\n"
"v_mfma_f32_16x16x32_bf16 a[180:183], a[244:247], v[148:151], a[180:183]\n"
"v_exp_f32_e32 v212, v212\n"
"v_mfma_f32_16x16x32_bf16 a[180:183], a[240:243], v[132:135], a[180:183]\n"
"v_exp_f32_e32 v213, v213\n"
"v_mfma_f32_16x16x32_bf16 v[28:31], a[240:243], v[136:139], v[28:31]\n"
"ds_read_b64_tr_b16 a[236:237], v243 offset:256\n"
"ds_read_b64_tr_b16 a[238:239], v243 offset:6400\n"
"v_mfma_f32_16x16x32_bf16 v[28:31], a[244:247], v[152:155], v[28:31]\n"
"v_cvt_pk_bf16_f32 v182, v212, v213\n"
"v_add_f32_e32 v250, v250, v212\n"
"v_mfma_f32_16x16x32_bf16 v[60:63], a[244:247], v[156:159], v[60:63]\n"
"v_exp_f32_e32 v214, v214\n"
"v_mfma_f32_16x16x32_bf16 v[60:63], a[240:243], v[140:143], v[60:63]\n"
"v_add_f32_e32 v250, v250, v213\n"
"v_add_f32_e32 v250, v250, v214\n"
"v_mfma_f32_16x16x32_bf16 v[108:111], a[240:243], v[224:227], v[108:111]\n"
"s_waitcnt lgkmcnt(4)\n"
"v_exp_f32_e32 v215, v215\n"
"v_mfma_f32_16x16x32_bf16 a[184:187], a[248:251], v[128:131], a[184:187]\n"
"ds_read_b64_tr_b16 a[240:241], v240 offset:256\n"
"ds_read_b64_tr_b16 a[242:243], v240 offset:6400\n"
"v_mfma_f32_16x16x32_bf16 a[184:187], a[252:255], v[144:147], a[184:187]\n"
"v_cvt_pk_bf16_f32 v183, v214, v215\n"
"v_add_f32_e32 v250, v250, v215\n"
"v_mfma_f32_16x16x32_bf16 a[188:191], a[252:255], v[148:151], a[188:191]\n"
"v_exp_f32_e32 v216, v216\n"
"v_mfma_f32_16x16x32_bf16 a[188:191], a[248:251], v[132:135], a[188:191]\n"
"v_exp_f32_e32 v217, v217\n"
"v_mfma_f32_16x16x32_bf16 a[216:219], a[248:251], v[136:139], a[216:219]\n"
"ds_read_b64_tr_b16 a[244:245], v244 offset:256\n"
"ds_read_b64_tr_b16 a[246:247], v244 offset:6400\n"
"v_mfma_f32_16x16x32_bf16 a[216:219], a[252:255], v[152:155], a[216:219]\n"
"s_waitcnt vmcnt(6)\n"
"buffer_load_dwordx4 v248, s[40:43], s56 offen offset:2048 nt lds\n"
"v_mfma_f32_16x16x32_bf16 v[64:67], a[252:255], v[156:159], v[64:67]\n"
"s_add_u32 s55, s55, 24576\n"
"v_cvt_pk_bf16_f32 v186, v216, v217\n"
"v_add_f32_e32 v251, v251, v216\n"
"v_mfma_f32_16x16x32_bf16 v[64:67], a[248:251], v[140:143], v[64:67]\n"
"s_add_u32 s56, s56, 24576\n"
"v_exp_f32_e32 v218, v218\n"
"v_mfma_f32_16x16x32_bf16 v[112:115], a[248:251], v[224:227], v[112:115]\n"
"s_waitcnt lgkmcnt(4)\n"
"v_add_f32_e32 v251, v251, v217\n"
"v_add_f32_e32 v251, v251, v218\n"
"v_mfma_f32_16x16x32_bf16 a[192:195], a[232:235], v[128:131], a[192:195]\n"
"s_mov_b32 s79, s57\n"
"ds_read_b64_tr_b16 a[248:249], v241 offset:256\n"
"ds_read_b64_tr_b16 a[250:251], v241 offset:6400\n"
"v_mfma_f32_16x16x32_bf16 a[192:195], a[236:239], v[144:147], a[192:195]\n"
"s_add_u32 s57, s57, 24576\n"
"ds_read_b64_tr_b16 a[252:253], v245 offset:256\n"
"ds_read_b64_tr_b16 a[254:255], v245 offset:6400\n"
"v_mfma_f32_16x16x32_bf16 a[196:199], a[236:239], v[148:151], a[196:199]\n"
"s_cmp_eq_u32 s57, 73728\n"
"v_add_u32_e32 v238, s64, v238\n"
"v_add_u32_e32 v239, s64, v239\n"
"v_mfma_f32_16x16x32_bf16 a[196:199], a[232:235], v[132:135], a[196:199]\n"
"s_cselect_b32 s57, 0, s57\n"
"v_add_u32_e32 v240, s64, v240\n"
"v_add_u32_e32 v241, s64, v241\n"
"v_mfma_f32_16x16x32_bf16 a[220:223], a[232:235], v[136:139], a[220:223]\n"
"v_add_u32_e32 v242, s64, v242\n"
"v_add_u32_e32 v243, s64, v243\n"
"v_mfma_f32_16x16x32_bf16 a[220:223], a[236:239], v[152:155], a[220:223]\n"
"v_add_u32_e32 v244, s64, v244\n"
"v_add_u32_e32 v245, s64, v245\n"
"v_mfma_f32_16x16x32_bf16 v[68:71], a[236:239], v[156:159], v[68:71]\n"
"v_exp_f32_e32 v219, v219\n"
"v_mfma_f32_16x16x32_bf16 v[68:71], a[232:235], v[140:143], v[68:71]\n"
"v_cvt_pk_bf16_f32 v187, v218, v219\n"
"v_add_f32_e32 v251, v251, v219\n"
"v_mfma_f32_16x16x32_bf16 v[116:119], a[232:235], v[224:227], v[116:119]\n"
"s_waitcnt vmcnt(12) lgkmcnt(0)\n"
"s_barrier\n"
"v_mfma_f32_16x16x32_bf16 a[200:203], a[240:243], v[128:131], a[200:203]\n"
"ds_read_b128 a[232:235], v236 offset:0\n"
"v_mfma_f32_16x16x32_bf16 a[200:203], a[244:247], v[144:147], a[200:203]\n"
"v_exp_f32_e32 v220, v220\n"
"v_mfma_f32_16x16x32_bf16 a[204:207], a[244:247], v[148:151], a[204:207]\n"
"s_cmp_lg_u32 s55, 245760\n"
"v_exp_f32_e32 v221, v221\n"
"v_mfma_f32_16x16x32_bf16 a[204:207], a[240:243], v[132:135], a[204:207]\n"
"v_cvt_pk_bf16_f32 v190, v220, v221\n"
"v_add_f32_e32 v252, v252, v220\n"
"v_mfma_f32_16x16x32_bf16 a[224:227], a[240:243], v[136:139], a[224:227]\n"
"ds_read_b128 a[236:239], v237 offset:0\n"
"v_add_f32_e32 v252, v252, v221\n"
"v_mfma_f32_16x16x32_bf16 a[224:227], a[244:247], v[152:155], a[224:227]\n"
"v_exp_f32_e32 v222, v222\n"
"v_mfma_f32_16x16x32_bf16 v[72:75], a[244:247], v[156:159], v[72:75]\n"
"v_exp_f32_e32 v223, v223\n"
"v_mfma_f32_16x16x32_bf16 v[72:75], a[240:243], v[140:143], v[72:75]\n"
"v_cvt_pk_bf16_f32 v191, v222, v223\n"
"v_add_f32_e32 v252, v252, v222\n"
"v_mfma_f32_16x16x32_bf16 v[120:123], a[240:243], v[224:227], v[120:123]\n"
"v_add_f32_e32 v252, v252, v223\n"
"v_mfma_f32_16x16x32_bf16 a[208:211], a[248:251], v[128:131], a[208:211]\n"
"ds_read_b128 a[240:243], v236 offset:128\n"
"v_mfma_f32_16x16x32_bf16 a[208:211], a[252:255], v[144:147], a[208:211]\n"
"v_mfma_f32_16x16x32_bf16 a[212:215], a[252:255], v[148:151], a[212:215]\n"
"v_mfma_f32_16x16x32_bf16 a[212:215], a[248:251], v[132:135], a[212:215]\n"
"v_mfma_f32_16x16x32_bf16 a[228:231], a[248:251], v[136:139], a[228:231]\n"
"ds_read_b128 a[244:247], v237 offset:128\n"
"v_mfma_f32_16x16x32_bf16 a[228:231], a[252:255], v[152:155], a[228:231]\n"
"v_mfma_f32_16x16x32_bf16 v[76:79], a[252:255], v[156:159], v[76:79]\n"
"v_mfma_f32_16x16x32_bf16 v[76:79], a[248:251], v[140:143], v[76:79]\n"
"v_mfma_f32_16x16x32_bf16 v[124:127], a[248:251], v[224:227], v[124:127]\n"
"s_cbranch_scc1 .Lloop_%=\n"
".LF_%=:\n"
"s_mul_i32 s69, s67, 24576\n"
"s_add_u32 s69, s69, s72\n"
"s_lshl_b32 s70, s68, 5\n"
"s_mul_i32 s71, s67, 6144\n"
"s_add_u32 s71, s71, s72\n"
"s_add_u32 s71, s71, 98304\n"
"s_lshl_b32 s73, s67, 8\n"
"s_add_u32 s73, s73, s72\n"
"s_add_u32 s73, s73, 147456\n"
"s_mul_i32 s78, %2, 110592\n"
"s_add_u32 s48, s38, s78\n"
"s_addc_u32 s49, s39, 0\n"
"s_and_b32 s49, s49, 0xffff\n"
"s_mul_i32 s42, s67, 24576\n"
"s_add_u32 s43, s42, 6144\n"
"s_add_u32 s44, s42, 12288\n"
"s_add_u32 s45, s42, 18432\n"
"s_mov_b32 s40, 0x30800000\n"
"s_mov_b32 s41, 0x53800000\n"
"v_mbcnt_lo_u32_b32 v128, -1, 0\n"
"v_mbcnt_hi_u32_b32 v128, -1, v128\n"
"v_and_b32_e32 v151, 15, v128\n"
"v_bfe_u32 v143, v128, 5, 1\n"
"v_bfrev_b32_e32 v152, v151\n"
"v_mul_u32_u24_e32 v151, 0x180, v151\n"
"v_bfe_u32 v152, v152, 28, 3\n"
"v_bfe_u32 v144, v128, 4, 1\n"
"v_xor_b32_e32 v143, v143, v152\n"
"v_lshlrev_b32_e32 v144, 3, v144\n"
"v_lshlrev_b32_e32 v143, 4, v143\n"
"v_add_u32_e32 v151, v151, v144\n"
"v_add_u32_e32 v151, v151, v143\n"
"v_add_u32_e32 v142, s69, v151\n"
"v_xor_b32_e32 v142, s70, v142\n"
"v_xor_b32_e32 v143, 32, v142\n"
"v_xor_b32_e32 v144, 64, v142\n"
"v_xor_b32_e32 v145, 96, v142\n"
"s_waitcnt vmcnt(6)\n"
"s_barrier\n"
"s_waitcnt lgkmcnt(3)\n"
"ds_read_b64_tr_b16 a[232:233], v238 offset:0\n"
"ds_read_b64_tr_b16 a[234:235], v238 offset:6144\n"
"s_waitcnt lgkmcnt(4)\n"
"ds_read_b64_tr_b16 a[236:237], v242 offset:0\n"
"ds_read_b64_tr_b16 a[238:239], v242 offset:6144\n"
"s_waitcnt lgkmcnt(5)\n"
"ds_read_b64_tr_b16 a[240:241], v239 offset:0\n"
"ds_read_b64_tr_b16 a[242:243], v239 offset:6144\n"
"s_waitcnt lgkmcnt(6)\n"
"ds_read_b64_tr_b16 a[244:245], v243 offset:0\n"
"ds_read_b64_tr_b16 a[246:247], v243 offset:6144\n"
"s_waitcnt lgkmcnt(4)\n"
"v_mfma_f32_16x16x32_bf16 a[120:123], a[232:235], v[160:163], a[120:123]\n"
"ds_read_b64_tr_b16 a[248:249], v240 offset:0\n"
"ds_read_b64_tr_b16 a[250:251], v240 offset:6144\n"
"v_mfma_f32_16x16x32_bf16 a[120:123], a[236:239], v[176:179], a[120:123]\n"
"v_lshrrev_b32_e32 v153, 3, v128\n"
"v_and_b32_e32 v152, 7, v128\n"
"v_mfma_f32_16x16x32_bf16 a[124:127], a[236:239], v[180:183], a[124:127]\n"
"v_bfrev_b32_e32 v148, v153\n"
"v_mul_u32_u24_e32 v153, 0x180, v153\n"
"v_mfma_f32_16x16x32_bf16 a[124:127], a[232:235], v[164:167], a[124:127]\n"
"v_bfe_u32 v148, v148, 28, 3\n"
"v_lshlrev_b32_e32 v146, 4, v152\n"
"v_mfma_f32_16x16x32_bf16 v[0:3], a[232:235], v[168:171], v[0:3]\n"
"ds_read_b64_tr_b16 a[252:253], v244 offset:0\n"
"ds_read_b64_tr_b16 a[254:255], v244 offset:6144\n"
"v_mfma_f32_16x16x32_bf16 v[0:3], a[236:239], v[184:187], v[0:3]\n"
"v_xor_b32_e32 v148, v148, v152\n"
"v_add_u32_e32 v146, v146, v153\n"
"v_mfma_f32_16x16x32_bf16 v[32:35], a[236:239], v[188:191], v[32:35]\n"
"v_lshlrev_b32_e32 v148, 4, v148\n"
"v_add_u32_e32 v146, s69, v146\n"
"v_mfma_f32_16x16x32_bf16 v[32:35], a[232:235], v[172:175], v[32:35]\n"
"v_add_u32_e32 v147, v148, v153\n"
"v_lshlrev_b32_e32 v149, 4, v128\n"
"v_mfma_f32_16x16x32_bf16 v[80:83], a[232:235], v[228:231], v[80:83]\n"
"s_waitcnt lgkmcnt(4)\n"
"v_xor_b32_e32 v148, 16, v147\n"
"v_add_u32_e32 v149, s71, v149\n"
"v_mfma_f32_16x16x32_bf16 a[128:131], a[240:243], v[160:163], a[128:131]\n"
"ds_read_b64_tr_b16 a[232:233], v241 offset:0\n"
"ds_read_b64_tr_b16 a[234:235], v241 offset:6144\n"
"v_mfma_f32_16x16x32_bf16 a[128:131], a[244:247], v[176:179], a[128:131]\n"
"v_lshlrev_b32_e32 v150, 2, v128\n"
"v_add_u32_e32 v150, s73, v150\n"
"v_mfma_f32_16x16x32_bf16 a[132:135], a[244:247], v[180:183], a[132:135]\n"
"v_permlane32_swap_b32 v249, v250\n"
"v_permlane32_swap_b32 v251, v252\n"
"v_mfma_f32_16x16x32_bf16 a[132:135], a[240:243], v[164:167], a[132:135]\n"
"v_add_f32_e32 v129, v249, v250\n"
"v_add_f32_e32 v130, v251, v252\n"
"v_mfma_f32_16x16x32_bf16 v[4:7], a[240:243], v[168:171], v[4:7]\n"
"ds_read_b64_tr_b16 a[236:237], v245 offset:0\n"
"ds_read_b64_tr_b16 a[238:239], v245 offset:6144\n"
"v_mfma_f32_16x16x32_bf16 v[4:7], a[244:247], v[184:187], v[4:7]\n"
"v_permlane16_swap_b32 v129, v130\n"
"v_mfma_f32_16x16x32_bf16 v[36:39], a[244:247], v[188:191], v[36:39]\n"
"v_add_f32_e32 v129, v129, v130\n"
"v_mfma_f32_16x16x32_bf16 v[36:39], a[240:243], v[172:175], v[36:39]\n"
"v_rcp_f32_e32 v130, v129\n"
"v_mfma_f32_16x16x32_bf16 v[84:87], a[240:243], v[228:231], v[84:87]\n"
"s_waitcnt vmcnt(0)\n"
"s_barrier\n"
"s_waitcnt lgkmcnt(4)\n"
"v_mfma_f32_16x16x32_bf16 a[136:139], a[248:251], v[160:163], a[136:139]\n"
"ds_read_b64_tr_b16 a[240:241], v238 offset:128\n"
"ds_read_b64_tr_b16 a[242:243], v238 offset:6272\n"
"v_mfma_f32_16x16x32_bf16 a[136:139], a[252:255], v[176:179], a[136:139]\n"
"v_cmp_nle_f32 vcc, s40, v129\n"
"v_mfma_f32_16x16x32_bf16 a[140:143], a[252:255], v[180:183], a[140:143]\n"
"v_mov_b32_e32 v132, v130\n"
"v_mov_b32_e32 v136, v130\n"
"v_mfma_f32_16x16x32_bf16 a[140:143], a[248:251], v[164:167], a[140:143]\n"
"s_mov_b64 s[74:75], vcc\n"
"v_mfma_f32_16x16x32_bf16 v[8:11], a[248:251], v[168:171], v[8:11]\n"
"ds_read_b64_tr_b16 a[244:245], v242 offset:128\n"
"ds_read_b64_tr_b16 a[246:247], v242 offset:6272\n"
"v_mfma_f32_16x16x32_bf16 v[8:11], a[252:255], v[184:187], v[8:11]\n"
"v_permlane16_swap_b32 v132, v136\n"
"v_cmp_nge_f32 vcc, s41, v129\n"
"v_mfma_f32_16x16x32_bf16 v[40:43], a[252:255], v[188:191], v[40:43]\n"
"v_mov_b32_e32 v138, v136\n"
"v_mov_b32_e32 v134, v132\n"
"v_mfma_f32_16x16x32_bf16 v[40:43], a[248:251], v[172:175], v[40:43]\n"
"s_or_b64 s[74:75], s[74:75], vcc\n"
"v_mfma_f32_16x16x32_bf16 v[88:91], a[248:251], v[228:231], v[88:91]\n"
"s_waitcnt lgkmcnt(4)\n"
"v_permlane32_swap_b32 v136, v138\n"
"v_permlane32_swap_b32 v132, v134\n"
"v_mfma_f32_16x16x32_bf16 a[144:147], a[232:235], v[160:163], a[144:147]\n"
"ds_read_b64_tr_b16 a[248:249], v239 offset:128\n"
"ds_read_b64_tr_b16 a[250:251], v239 offset:6272\n"
"v_mfma_f32_16x16x32_bf16 a[144:147], a[236:239], v[176:179], a[144:147]\n"
"v_mul_f32_e32 v0, v0, v136\n"
"v_mul_f32_e32 v1, v1, v136\n"
"v_mfma_f32_16x16x32_bf16 a[148:151], a[236:239], v[180:183], a[148:151]\n"
"v_mul_f32_e32 v2, v2, v136\n"
"v_mul_f32_e32 v3, v3, v136\n"
"v_mfma_f32_16x16x32_bf16 a[148:151], a[232:235], v[164:167], a[148:151]\n"
"v_mul_f32_e32 v4, v4, v136\n"
"v_mul_f32_e32 v5, v5, v136\n"
"v_mfma_f32_16x16x32_bf16 v[12:15], a[232:235], v[168:171], v[12:15]\n"
"ds_read_b64_tr_b16 a[252:253], v243 offset:128\n"
"ds_read_b64_tr_b16 a[254:255], v243 offset:6272\n"
"v_mfma_f32_16x16x32_bf16 v[12:15], a[236:239], v[184:187], v[12:15]\n"
"v_mul_f32_e32 v6, v6, v136\n"
"v_mul_f32_e32 v7, v7, v136\n"
"v_mfma_f32_16x16x32_bf16 v[44:47], a[236:239], v[188:191], v[44:47]\n"
"v_cvt_pk_bf16_f32 v0, v0, v1\n"
"v_cvt_pk_bf16_f32 v1, v2, v3\n"
"v_mfma_f32_16x16x32_bf16 v[44:47], a[232:235], v[172:175], v[44:47]\n"
"v_cvt_pk_bf16_f32 v4, v4, v5\n"
"v_cvt_pk_bf16_f32 v5, v6, v7\n"
"v_mfma_f32_16x16x32_bf16 v[92:95], a[232:235], v[228:231], v[92:95]\n"
"s_waitcnt lgkmcnt(4)\n"
"ds_write_b64 v142, v[0:1] offset:12288\n"
"ds_write_b64 v143, v[4:5] offset:12288\n"
"v_mfma_f32_16x16x32_bf16 a[152:155], a[240:243], v[160:163], a[152:155]\n"
"ds_read_b64_tr_b16 a[232:233], v240 offset:128\n"
"ds_read_b64_tr_b16 a[234:235], v240 offset:6272\n"
"v_mfma_f32_16x16x32_bf16 a[152:155], a[244:247], v[176:179], a[152:155]\n"
"v_mfma_f32_16x16x32_bf16 a[156:159], a[244:247], v[180:183], a[156:159]\n"
"v_mfma_f32_16x16x32_bf16 a[156:159], a[240:243], v[164:167], a[156:159]\n"
"v_mfma_f32_16x16x32_bf16 v[16:19], a[240:243], v[168:171], v[16:19]\n"
"ds_read_b64_tr_b16 a[236:237], v244 offset:128\n"
"ds_read_b64_tr_b16 a[238:239], v244 offset:6272\n"
"v_mfma_f32_16x16x32_bf16 v[16:19], a[244:247], v[184:187], v[16:19]\n"
"v_mul_f32_e32 v8, v8, v136\n"
"v_mul_f32_e32 v9, v9, v136\n"
"v_mfma_f32_16x16x32_bf16 v[48:51], a[244:247], v[188:191], v[48:51]\n"
"v_mul_f32_e32 v10, v10, v136\n"
"v_mul_f32_e32 v11, v11, v136\n"
"v_mfma_f32_16x16x32_bf16 v[48:51], a[240:243], v[172:175], v[48:51]\n"
"v_mul_f32_e32 v12, v12, v136\n"
"v_mul_f32_e32 v13, v13, v136\n"
"v_mfma_f32_16x16x32_bf16 v[96:99], a[240:243], v[228:231], v[96:99]\n"
"s_waitcnt lgkmcnt(6)\n"
"v_mul_f32_e32 v14, v14, v136\n"
"v_mul_f32_e32 v15, v15, v136\n"
"v_mfma_f32_16x16x32_bf16 a[160:163], a[248:251], v[160:163], a[160:163]\n"
"ds_read_b64_tr_b16 a[240:241], v241 offset:128\n"
"ds_read_b64_tr_b16 a[242:243], v241 offset:6272\n"
"v_mfma_f32_16x16x32_bf16 a[160:163], a[252:255], v[176:179], a[160:163]\n"
"v_cvt_pk_bf16_f32 v8, v8, v9\n"
"v_cvt_pk_bf16_f32 v9, v10, v11\n"
"v_mfma_f32_16x16x32_bf16 a[164:167], a[252:255], v[180:183], a[164:167]\n"
"v_cvt_pk_bf16_f32 v12, v12, v13\n"
"v_cvt_pk_bf16_f32 v13, v14, v15\n"
"v_mfma_f32_16x16x32_bf16 a[164:167], a[248:251], v[164:167], a[164:167]\n"
"ds_write_b64 v144, v[8:9] offset:12288\n"
"ds_write_b64 v145, v[12:13] offset:12288\n"
"v_mfma_f32_16x16x32_bf16 v[20:23], a[248:251], v[168:171], v[20:23]\n"
"ds_read_b64_tr_b16 a[244:245], v245 offset:128\n"
"ds_read_b64_tr_b16 a[246:247], v245 offset:6272\n"
"v_mfma_f32_16x16x32_bf16 v[20:23], a[252:255], v[184:187], v[20:23]\n"
"ds_read_b128 v[192:195], v146 offset:12288\n"
"ds_read_b128 v[196:199], v146 offset:15360\n"
"v_mfma_f32_16x16x32_bf16 v[52:55], a[252:255], v[188:191], v[52:55]\n"
"v_mul_f32_e32 v32, v32, v138\n"
"v_mul_f32_e32 v33, v33, v138\n"
"v_mfma_f32_16x16x32_bf16 v[52:55], a[248:251], v[172:175], v[52:55]\n"
"v_mul_f32_e32 v34, v34, v138\n"
"v_mul_f32_e32 v35, v35, v138\n"
"v_mfma_f32_16x16x32_bf16 v[100:103], a[248:251], v[228:231], v[100:103]\n"
"s_waitcnt lgkmcnt(8)\n"
"v_mul_f32_e32 v36, v36, v138\n"
"v_mul_f32_e32 v37, v37, v138\n"
"v_mfma_f32_16x16x32_bf16 a[168:171], a[232:235], v[160:163], a[168:171]\n"
"ds_read_b64_tr_b16 a[248:249], v238 offset:256\n"
"ds_read_b64_tr_b16 a[250:251], v238 offset:6400\n"
"v_mfma_f32_16x16x32_bf16 a[168:171], a[236:239], v[176:179], a[168:171]\n"
"v_mul_f32_e32 v38, v38, v138\n"
"v_mul_f32_e32 v39, v39, v138\n"
"v_mfma_f32_16x16x32_bf16 a[172:175], a[236:239], v[180:183], a[172:175]\n"
"v_cvt_pk_bf16_f32 v32, v32, v33\n"
"v_cvt_pk_bf16_f32 v33, v34, v35\n"
"v_mfma_f32_16x16x32_bf16 a[172:175], a[232:235], v[164:167], a[172:175]\n"
"v_cvt_pk_bf16_f32 v36, v36, v37\n"
"v_cvt_pk_bf16_f32 v37, v38, v39\n"
"v_mfma_f32_16x16x32_bf16 v[24:27], a[232:235], v[168:171], v[24:27]\n"
"ds_read_b64_tr_b16 a[252:253], v242 offset:256\n"
"ds_read_b64_tr_b16 a[254:255], v242 offset:6400\n"
"v_mfma_f32_16x16x32_bf16 v[24:27], a[236:239], v[184:187], v[24:27]\n"
"ds_write_b64 v142, v[32:33] offset:18432\n"
"ds_write_b64 v143, v[36:37] offset:18432\n"
"v_mfma_f32_16x16x32_bf16 v[56:59], a[236:239], v[188:191], v[56:59]\n"
"s_waitcnt lgkmcnt(7)\n"
"buffer_store_dwordx4 v[192:195], v147, s[48:51], s44 offen offset:0 nt\n"
"v_mfma_f32_16x16x32_bf16 v[56:59], a[232:235], v[172:175], v[56:59]\n"
"s_waitcnt lgkmcnt(6)\n"
"buffer_store_dwordx4 v[196:199], v148, s[48:51], s44 offen offset:3072 nt\n"
"v_mfma_f32_16x16x32_bf16 v[104:107], a[232:235], v[228:231], v[104:107]\n"
"v_mul_f32_e32 v40, v40, v138\n"
"v_mul_f32_e32 v41, v41, v138\n"
"v_mfma_f32_16x16x32_bf16 a[176:179], a[240:243], v[160:163], a[176:179]\n"
"ds_read_b64_tr_b16 a[232:233], v239 offset:256\n"
"ds_read_b64_tr_b16 a[234:235], v239 offset:6400\n"
"v_mfma_f32_16x16x32_bf16 a[176:179], a[244:247], v[176:179], a[176:179]\n"
"v_mul_f32_e32 v42, v42, v138\n"
"v_mul_f32_e32 v43, v43, v138\n"
"v_mfma_f32_16x16x32_bf16 a[180:183], a[244:247], v[180:183], a[180:183]\n"
"v_mul_f32_e32 v44, v44, v138\n"
"v_mul_f32_e32 v45, v45, v138\n"
"v_mfma_f32_16x16x32_bf16 a[180:183], a[240:243], v[164:167], a[180:183]\n"
"v_mul_f32_e32 v46, v46, v138\n"
"v_mul_f32_e32 v47, v47, v138\n"
"v_mfma_f32_16x16x32_bf16 v[28:31], a[240:243], v[168:171], v[28:31]\n"
"ds_read_b64_tr_b16 a[236:237], v243 offset:256\n"
"ds_read_b64_tr_b16 a[238:239], v243 offset:6400\n"
"v_mfma_f32_16x16x32_bf16 v[28:31], a[244:247], v[184:187], v[28:31]\n"
"v_cvt_pk_bf16_f32 v40, v40, v41\n"
"v_cvt_pk_bf16_f32 v41, v42, v43\n"
"v_mfma_f32_16x16x32_bf16 v[60:63], a[244:247], v[188:191], v[60:63]\n"
"v_cvt_pk_bf16_f32 v44, v44, v45\n"
"v_cvt_pk_bf16_f32 v45, v46, v47\n"
"v_mfma_f32_16x16x32_bf16 v[60:63], a[240:243], v[172:175], v[60:63]\n"
"ds_write_b64 v144, v[40:41] offset:18432\n"
"ds_write_b64 v145, v[44:45] offset:18432\n"
"v_mfma_f32_16x16x32_bf16 v[108:111], a[240:243], v[228:231], v[108:111]\n"
"s_waitcnt lgkmcnt(8)\n"
"ds_read_b128 v[200:203], v146 offset:18432\n"
"ds_read_b128 v[204:207], v146 offset:21504\n"
"v_mfma_f32_16x16x32_bf16 a[184:187], a[248:251], v[160:163], a[184:187]\n"
"ds_read_b64_tr_b16 a[240:241], v240 offset:256\n"
"ds_read_b64_tr_b16 a[242:243], v240 offset:6400\n"
"v_mfma_f32_16x16x32_bf16 a[184:187], a[252:255], v[176:179], a[184:187]\n"
"v_mul_f32_e32 v16, v16, v136\n"
"v_mul_f32_e32 v17, v17, v136\n"
"v_mfma_f32_16x16x32_bf16 a[188:191], a[252:255], v[180:183], a[188:191]\n"
"v_mul_f32_e32 v18, v18, v136\n"
"v_mul_f32_e32 v19, v19, v136\n"
"v_mfma_f32_16x16x32_bf16 a[188:191], a[248:251], v[164:167], a[188:191]\n"
"v_mul_f32_e32 v20, v20, v136\n"
"v_mul_f32_e32 v21, v21, v136\n"
"v_mfma_f32_16x16x32_bf16 a[216:219], a[248:251], v[168:171], a[216:219]\n"
"ds_read_b64_tr_b16 a[244:245], v244 offset:256\n"
"ds_read_b64_tr_b16 a[246:247], v244 offset:6400\n"
"v_mfma_f32_16x16x32_bf16 a[216:219], a[252:255], v[184:187], a[216:219]\n"
"v_mul_f32_e32 v22, v22, v136\n"
"v_mul_f32_e32 v23, v23, v136\n"
"v_mfma_f32_16x16x32_bf16 v[64:67], a[252:255], v[188:191], v[64:67]\n"
"v_cvt_pk_bf16_f32 v16, v16, v17\n"
"v_cvt_pk_bf16_f32 v17, v18, v19\n"
"v_mfma_f32_16x16x32_bf16 v[64:67], a[248:251], v[172:175], v[64:67]\n"
"v_cvt_pk_bf16_f32 v20, v20, v21\n"
"v_cvt_pk_bf16_f32 v21, v22, v23\n"
"v_mfma_f32_16x16x32_bf16 v[112:115], a[248:251], v[228:231], v[112:115]\n"
"s_waitcnt lgkmcnt(8)\n"
"ds_write_b64 v142, v[16:17] offset:12416\n"
"ds_write_b64 v143, v[20:21] offset:12416\n"
"v_mfma_f32_16x16x32_bf16 a[192:195], a[232:235], v[160:163], a[192:195]\n"
"ds_read_b64_tr_b16 a[248:249], v241 offset:256\n"
"ds_read_b64_tr_b16 a[250:251], v241 offset:6400\n"
"v_mfma_f32_16x16x32_bf16 a[192:195], a[236:239], v[176:179], a[192:195]\n"
"s_waitcnt lgkmcnt(9)\n"
"buffer_store_dwordx4 v[200:203], v147, s[48:51], s45 offen offset:0 nt\n"
"v_mfma_f32_16x16x32_bf16 a[196:199], a[236:239], v[180:183], a[196:199]\n"
"s_waitcnt lgkmcnt(8)\n"
"buffer_store_dwordx4 v[204:207], v148, s[48:51], s45 offen offset:3072 nt\n"
"v_mfma_f32_16x16x32_bf16 a[196:199], a[232:235], v[164:167], a[196:199]\n"
"v_mul_f32_e32 v24, v24, v136\n"
"v_mul_f32_e32 v25, v25, v136\n"
"v_mfma_f32_16x16x32_bf16 a[220:223], a[232:235], v[168:171], a[220:223]\n"
"ds_read_b64_tr_b16 a[252:253], v245 offset:256\n"
"ds_read_b64_tr_b16 a[254:255], v245 offset:6400\n"
"v_mfma_f32_16x16x32_bf16 a[220:223], a[236:239], v[184:187], a[220:223]\n"
"v_mul_f32_e32 v26, v26, v136\n"
"v_mul_f32_e32 v27, v27, v136\n"
"v_mfma_f32_16x16x32_bf16 v[68:71], a[236:239], v[188:191], v[68:71]\n"
"v_mul_f32_e32 v28, v28, v136\n"
"v_mul_f32_e32 v29, v29, v136\n"
"v_mfma_f32_16x16x32_bf16 v[68:71], a[232:235], v[172:175], v[68:71]\n"
"v_mul_f32_e32 v30, v30, v136\n"
"v_mul_f32_e32 v31, v31, v136\n"
"v_mfma_f32_16x16x32_bf16 v[116:119], a[232:235], v[228:231], v[116:119]\n"
"s_waitcnt lgkmcnt(6)\n"
"v_cvt_pk_bf16_f32 v24, v24, v25\n"
"v_cvt_pk_bf16_f32 v25, v26, v27\n"
"v_mfma_f32_16x16x32_bf16 a[200:203], a[240:243], v[160:163], a[200:203]\n"
"v_cvt_pk_bf16_f32 v28, v28, v29\n"
"v_cvt_pk_bf16_f32 v29, v30, v31\n"
"v_mfma_f32_16x16x32_bf16 a[200:203], a[244:247], v[176:179], a[200:203]\n"
"ds_write_b64 v144, v[24:25] offset:12416\n"
"ds_write_b64 v145, v[28:29] offset:12416\n"
"v_mfma_f32_16x16x32_bf16 a[204:207], a[244:247], v[180:183], a[204:207]\n"
"ds_read_b128 v[208:211], v146 offset:12416\n"
"ds_read_b128 v[212:215], v146 offset:15488\n"
"v_mfma_f32_16x16x32_bf16 a[204:207], a[240:243], v[164:167], a[204:207]\n"
"v_mul_f32_e32 v48, v48, v138\n"
"v_mul_f32_e32 v49, v49, v138\n"
"v_mfma_f32_16x16x32_bf16 a[224:227], a[240:243], v[168:171], a[224:227]\n"
"v_mul_f32_e32 v50, v50, v138\n"
"v_mul_f32_e32 v51, v51, v138\n"
"v_mfma_f32_16x16x32_bf16 a[224:227], a[244:247], v[184:187], a[224:227]\n"
"v_mul_f32_e32 v52, v52, v138\n"
"v_mul_f32_e32 v53, v53, v138\n"
"v_mfma_f32_16x16x32_bf16 v[72:75], a[244:247], v[188:191], v[72:75]\n"
"v_mul_f32_e32 v54, v54, v138\n"
"v_mul_f32_e32 v55, v55, v138\n"
"v_mfma_f32_16x16x32_bf16 v[72:75], a[240:243], v[172:175], v[72:75]\n"
"v_cvt_pk_bf16_f32 v48, v48, v49\n"
"v_cvt_pk_bf16_f32 v49, v50, v51\n"
"v_mfma_f32_16x16x32_bf16 v[120:123], a[240:243], v[228:231], v[120:123]\n"
"s_waitcnt lgkmcnt(4)\n"
"v_cvt_pk_bf16_f32 v52, v52, v53\n"
"v_cvt_pk_bf16_f32 v53, v54, v55\n"
"v_mfma_f32_16x16x32_bf16 a[208:211], a[248:251], v[160:163], a[208:211]\n"
"ds_write_b64 v142, v[48:49] offset:18560\n"
"ds_write_b64 v143, v[52:53] offset:18560\n"
"v_mfma_f32_16x16x32_bf16 a[208:211], a[252:255], v[176:179], a[208:211]\n"
"s_waitcnt lgkmcnt(3)\n"
"buffer_store_dwordx4 v[208:211], v147, s[48:51], s44 offen offset:128 nt\n"
"v_mfma_f32_16x16x32_bf16 a[212:215], a[252:255], v[180:183], a[212:215]\n"
"s_waitcnt lgkmcnt(2)\n"
"buffer_store_dwordx4 v[212:215], v148, s[48:51], s44 offen offset:3200 nt\n"
"v_mfma_f32_16x16x32_bf16 a[212:215], a[248:251], v[164:167], a[212:215]\n"
"v_mul_f32_e32 v56, v56, v138\n"
"v_mul_f32_e32 v57, v57, v138\n"
"v_mfma_f32_16x16x32_bf16 a[228:231], a[248:251], v[168:171], a[228:231]\n"
"v_mul_f32_e32 v58, v58, v138\n"
"v_mul_f32_e32 v59, v59, v138\n"
"v_mfma_f32_16x16x32_bf16 a[228:231], a[252:255], v[184:187], a[228:231]\n"
"v_mul_f32_e32 v60, v60, v138\n"
"v_mul_f32_e32 v61, v61, v138\n"
"v_mfma_f32_16x16x32_bf16 v[76:79], a[252:255], v[188:191], v[76:79]\n"
"v_mul_f32_e32 v62, v62, v138\n"
"v_mul_f32_e32 v63, v63, v138\n"
"v_mfma_f32_16x16x32_bf16 v[76:79], a[248:251], v[172:175], v[76:79]\n"
"v_cvt_pk_bf16_f32 v56, v56, v57\n"
"v_cvt_pk_bf16_f32 v57, v58, v59\n"
"v_mfma_f32_16x16x32_bf16 v[124:127], a[248:251], v[228:231], v[124:127]\n"
"v_cvt_pk_bf16_f32 v60, v60, v61\n"
"v_cvt_pk_bf16_f32 v61, v62, v63\n"
"ds_write_b64 v144, v[56:57] offset:18560\n"
"ds_write_b64 v145, v[60:61] offset:18560\n"
"ds_read_b128 v[192:195], v146 offset:18560\n"
"ds_read_b128 v[196:199], v146 offset:21632\n"
"v_accvgpr_read_b32 v160, a216\n"
"v_accvgpr_read_b32 v161, a217\n"
"v_accvgpr_read_b32 v162, a218\n"
"v_accvgpr_read_b32 v163, a219\n"
"v_accvgpr_read_b32 v164, a220\n"
"v_accvgpr_read_b32 v165, a221\n"
"v_accvgpr_read_b32 v166, a222\n"
"v_accvgpr_read_b32 v167, a223\n"
"v_accvgpr_read_b32 v168, a224\n"
"v_accvgpr_read_b32 v169, a225\n"
"v_accvgpr_read_b32 v170, a226\n"
"v_accvgpr_read_b32 v171, a227\n"
"v_accvgpr_read_b32 v172, a228\n"
"v_accvgpr_read_b32 v173, a229\n"
"s_waitcnt lgkmcnt(1)\n"
"buffer_store_dwordx4 v[192:195], v147, s[48:51], s45 offen offset:128 nt\n"
"v_accvgpr_read_b32 v174, a230\n"
"v_accvgpr_read_b32 v175, a231\n"
"v_pk_mul_f32 v[160:161], v[160:161], v[136:137] op_sel_hi:[1,0]\n"
"v_pk_mul_f32 v[162:163], v[162:163], v[136:137] op_sel_hi:[1,0]\n"
"v_pk_mul_f32 v[164:165], v[164:165], v[136:137] op_sel_hi:[1,0]\n"
"v_pk_mul_f32 v[166:167], v[166:167], v[136:137] op_sel_hi:[1,0]\n"
"v_pk_mul_f32 v[168:169], v[168:169], v[136:137] op_sel_hi:[1,0]\n"
"v_pk_mul_f32 v[170:171], v[170:171], v[136:137] op_sel_hi:[1,0]\n"
"v_pk_mul_f32 v[172:173], v[172:173], v[136:137] op_sel_hi:[1,0]\n"
"v_pk_mul_f32 v[174:175], v[174:175], v[136:137] op_sel_hi:[1,0]\n"
"v_cvt_pk_bf16_f32 v160, v160, v161\n"
"v_cvt_pk_bf16_f32 v161, v162, v163\n"
"s_waitcnt lgkmcnt(0)\n"
"buffer_store_dwordx4 v[196:199], v148, s[48:51], s45 offen offset:3200 nt\n"
"v_cvt_pk_bf16_f32 v164, v164, v165\n"
"v_cvt_pk_bf16_f32 v165, v166, v167\n"
"v_cvt_pk_bf16_f32 v168, v168, v169\n"
"v_cvt_pk_bf16_f32 v169, v170, v171\n"
"v_cvt_pk_bf16_f32 v172, v172, v173\n"
"v_cvt_pk_bf16_f32 v173, v174, v175\n"
"v_accvgpr_read_b32 v176, a120\n"
"ds_write_b64 v142, v[160:161] offset:12544\n"
"v_accvgpr_read_b32 v177, a121\n"
"v_accvgpr_read_b32 v178, a122\n"
"v_accvgpr_read_b32 v179, a123\n"
"ds_write_b64 v143, v[164:165] offset:12544\n"
"v_accvgpr_read_b32 v180, a128\n"
"v_accvgpr_read_b32 v181, a129\n"
"v_accvgpr_read_b32 v182, a130\n"
"ds_write_b64 v144, v[168:169] offset:12544\n"
"v_accvgpr_read_b32 v183, a131\n"
"v_accvgpr_read_b32 v184, a136\n"
"v_accvgpr_read_b32 v185, a137\n"
"ds_write_b64 v145, v[172:173] offset:12544\n"
"v_accvgpr_read_b32 v186, a138\n"
"v_accvgpr_read_b32 v187, a139\n"
"v_accvgpr_read_b32 v188, a144\n"
"ds_read_b128 v[200:203], v146 offset:12544\n"
"v_accvgpr_read_b32 v189, a145\n"
"v_accvgpr_read_b32 v190, a146\n"
"v_accvgpr_read_b32 v191, a147\n"
"ds_read_b128 v[204:207], v146 offset:15616\n"
"v_pk_mul_f32 v[176:177], v[176:177], v[132:133] op_sel_hi:[1,0]\n"
"v_pk_mul_f32 v[178:179], v[178:179], v[132:133] op_sel_hi:[1,0]\n"
"v_pk_mul_f32 v[180:181], v[180:181], v[132:133] op_sel_hi:[1,0]\n"
"ds_write_b128 v149, v[84:87] offset:0\n"
"v_pk_mul_f32 v[182:183], v[182:183], v[132:133] op_sel_hi:[1,0]\n"
"v_pk_mul_f32 v[184:185], v[184:185], v[132:133] op_sel_hi:[1,0]\n"
"v_pk_mul_f32 v[186:187], v[186:187], v[132:133] op_sel_hi:[1,0]\n"
"ds_write_b128 v149, v[92:95] offset:1024\n"
"v_pk_mul_f32 v[188:189], v[188:189], v[132:133] op_sel_hi:[1,0]\n"
"v_pk_mul_f32 v[190:191], v[190:191], v[132:133] op_sel_hi:[1,0]\n"
"v_cvt_pk_bf16_f32 v176, v176, v177\n"
"ds_write_b128 v149, v[100:103] offset:2048\n"
"v_cvt_pk_bf16_f32 v177, v178, v179\n"
"v_cvt_pk_bf16_f32 v180, v180, v181\n"
"v_cvt_pk_bf16_f32 v181, v182, v183\n"
"ds_write_b128 v149, v[108:111] offset:3072\n"
"v_cvt_pk_bf16_f32 v184, v184, v185\n"
"v_cvt_pk_bf16_f32 v185, v186, v187\n"
"v_cvt_pk_bf16_f32 v188, v188, v189\n"
"ds_write_b128 v149, v[116:119] offset:4096\n"
"v_cvt_pk_bf16_f32 v189, v190, v191\n"
"v_pk_mul_f32 v[64:65], v[64:65], v[138:139] op_sel_hi:[1,0]\n"
"s_waitcnt lgkmcnt(6)\n"
"buffer_store_dwordx4 v[200:203], v147, s[48:51], s44 offen offset:256 nt\n"
"v_pk_mul_f32 v[66:67], v[66:67], v[138:139] op_sel_hi:[1,0]\n"
"ds_write_b128 v149, v[124:127] offset:5120\n"
"v_pk_mul_f32 v[68:69], v[68:69], v[138:139] op_sel_hi:[1,0]\n"
"v_pk_mul_f32 v[70:71], v[70:71], v[138:139] op_sel_hi:[1,0]\n"
"v_pk_mul_f32 v[72:73], v[72:73], v[138:139] op_sel_hi:[1,0]\n"
"ds_write_b32 v150, v253 offset:0\n"
"v_pk_mul_f32 v[74:75], v[74:75], v[138:139] op_sel_hi:[1,0]\n"
"v_pk_mul_f32 v[76:77], v[76:77], v[138:139] op_sel_hi:[1,0]\n"
"v_pk_mul_f32 v[78:79], v[78:79], v[138:139] op_sel_hi:[1,0]\n"
"ds_write_b64 v142, v[176:177] offset:0\n"
"v_cvt_pk_bf16_f32 v64, v64, v65\n"
"v_cvt_pk_bf16_f32 v65, v66, v67\n"
"v_cvt_pk_bf16_f32 v68, v68, v69\n"
"ds_write_b64 v143, v[180:181] offset:0\n"
"v_cvt_pk_bf16_f32 v69, v70, v71\n"
"v_cvt_pk_bf16_f32 v72, v72, v73\n"
"s_waitcnt lgkmcnt(9)\n"
"buffer_store_dwordx4 v[204:207], v148, s[48:51], s44 offen offset:3328 nt\n"
"v_cvt_pk_bf16_f32 v73, v74, v75\n"
"ds_write_b64 v144, v[184:185] offset:0\n"
"v_cvt_pk_bf16_f32 v76, v76, v77\n"
"v_cvt_pk_bf16_f32 v77, v78, v79\n"
"v_accvgpr_read_b32 v160, a124\n"
"ds_write_b64 v145, v[188:189] offset:0\n"
"v_accvgpr_read_b32 v161, a125\n"
"v_accvgpr_read_b32 v162, a126\n"
"v_accvgpr_read_b32 v163, a127\n"
"ds_read_b128 v[208:211], v146 offset:0\n"
"v_accvgpr_read_b32 v164, a132\n"
"v_accvgpr_read_b32 v165, a133\n"
"v_accvgpr_read_b32 v166, a134\n"
"ds_read_b128 v[212:215], v146 offset:3072\n"
"v_accvgpr_read_b32 v167, a135\n"
"v_accvgpr_read_b32 v168, a140\n"
"v_accvgpr_read_b32 v169, a141\n"
"ds_write_b64 v142, v[64:65] offset:18688\n"
"v_accvgpr_read_b32 v170, a142\n"
"v_accvgpr_read_b32 v171, a143\n"
"v_accvgpr_read_b32 v172, a148\n"
"ds_write_b64 v143, v[68:69] offset:18688\n"
"v_accvgpr_read_b32 v173, a149\n"
"v_accvgpr_read_b32 v174, a150\n"
"v_accvgpr_read_b32 v175, a151\n"
"s_waitcnt lgkmcnt(14)\n"
"ds_write_b64 v144, v[72:73] offset:18688\n"
"v_pk_mul_f32 v[160:161], v[160:161], v[134:135] op_sel_hi:[1,0]\n"
"v_pk_mul_f32 v[162:163], v[162:163], v[134:135] op_sel_hi:[1,0]\n"
"v_pk_mul_f32 v[164:165], v[164:165], v[134:135] op_sel_hi:[1,0]\n"
"s_waitcnt lgkmcnt(14)\n"
"ds_write_b64 v145, v[76:77] offset:18688\n"
"v_pk_mul_f32 v[166:167], v[166:167], v[134:135] op_sel_hi:[1,0]\n"
"v_pk_mul_f32 v[168:169], v[168:169], v[134:135] op_sel_hi:[1,0]\n"
"v_pk_mul_f32 v[170:171], v[170:171], v[134:135] op_sel_hi:[1,0]\n"
"s_waitcnt lgkmcnt(14)\n"
"ds_read_b128 v[192:195], v146 offset:18688\n"
"v_pk_mul_f32 v[172:173], v[172:173], v[134:135] op_sel_hi:[1,0]\n"
"v_pk_mul_f32 v[174:175], v[174:175], v[134:135] op_sel_hi:[1,0]\n"
"s_waitcnt lgkmcnt(6)\n"
"buffer_store_dwordx4 v[208:211], v147, s[48:51], s42 offen offset:0 nt\n"
"v_cvt_pk_bf16_f32 v160, v160, v161\n"
"ds_read_b128 v[196:199], v146 offset:21760\n"
"v_cvt_pk_bf16_f32 v161, v162, v163\n"
"v_cvt_pk_bf16_f32 v164, v164, v165\n"
"v_cvt_pk_bf16_f32 v165, v166, v167\n"
"v_cvt_pk_bf16_f32 v168, v168, v169\n"
"v_cvt_pk_bf16_f32 v169, v170, v171\n"
"v_cvt_pk_bf16_f32 v172, v172, v173\n"
"v_cvt_pk_bf16_f32 v173, v174, v175\n"
"s_waitcnt lgkmcnt(0)\n"
"s_barrier\n"
"s_lshl_b32 s69, s68, 1\n"
"s_sub_u32 s69, 1, s69\n"
"s_mul_i32 s70, s69, 12288\n"
"s_mul_i32 s71, s69, 512\n"
"v_add_u32_e32 v149, s70, v149\n"
"v_add_u32_e32 v150, s71, v150\n"
"ds_read_b32 v151, v150 offset:0\n"
"ds_read_b128 v[216:219], v149 offset:0\n"
"ds_read_b128 v[220:223], v149 offset:1024\n"
"ds_read_b128 v[224:227], v149 offset:2048\n"
"ds_read_b128 v[228:231], v149 offset:3072\n"
"ds_read_b128 v[232:235], v149 offset:4096\n"
"ds_read_b128 v[236:239], v149 offset:5120\n"
"v_and_b32_e32 v153, 15, v128\n"
"v_mul_u32_u24_e32 v153, 0x180, v153\n"
"v_lshrrev_b32_e32 v152, 4, v128\n"
"v_lshlrev_b32_e32 v152, 3, v152\n"
"v_add_u32_e32 v154, v153, v152\n"
"s_and_b32 s69, s67, 1\n"
"s_mul_i32 s69, s69, 6144\n"
"s_lshl_b32 s70, s68, 5\n"
"s_add_u32 s69, s69, s70\n"
"s_add_u32 s69, s69, 98304\n"
"v_accvgpr_read_b32 v176, a152\n"
"buffer_store_dwordx4 v[212:215], v148, s[48:51], s42 offen offset:3072 nt\n"
"v_accvgpr_read_b32 v177, a153\n"
"ds_write_b64 v142, v[160:161] offset:6144\n"
"v_accvgpr_read_b32 v178, a154\n"
"v_accvgpr_read_b32 v179, a155\n"
"v_accvgpr_read_b32 v180, a160\n"
"ds_write_b64 v143, v[164:165] offset:6144\n"
"v_accvgpr_read_b32 v181, a161\n"
"v_accvgpr_read_b32 v182, a162\n"
"v_accvgpr_read_b32 v183, a163\n"
"ds_write_b64 v144, v[168:169] offset:6144\n"
"v_accvgpr_read_b32 v184, a168\n"
"v_accvgpr_read_b32 v185, a169\n"
"v_accvgpr_read_b32 v186, a170\n"
"ds_write_b64 v145, v[172:173] offset:6144\n"
"v_accvgpr_read_b32 v187, a171\n"
"v_accvgpr_read_b32 v188, a176\n"
"buffer_store_dwordx4 v[192:195], v147, s[48:51], s45 offen offset:256 nt\n"
"v_accvgpr_read_b32 v189, a177\n"
"ds_read_b128 v[200:203], v146 offset:6144\n"
"v_accvgpr_read_b32 v190, a178\n"
"v_accvgpr_read_b32 v191, a179\n"
"v_pk_mul_f32 v[176:177], v[176:177], v[132:133] op_sel_hi:[1,0]\n"
"ds_read_b128 v[204:207], v146 offset:9216\n"
"v_pk_mul_f32 v[178:179], v[178:179], v[132:133] op_sel_hi:[1,0]\n"
"v_pk_mul_f32 v[180:181], v[180:181], v[132:133] op_sel_hi:[1,0]\n"
"v_pk_mul_f32 v[182:183], v[182:183], v[132:133] op_sel_hi:[1,0]\n"
"v_pk_mul_f32 v[184:185], v[184:185], v[132:133] op_sel_hi:[1,0]\n"
"v_pk_mul_f32 v[186:187], v[186:187], v[132:133] op_sel_hi:[1,0]\n"
"v_pk_mul_f32 v[188:189], v[188:189], v[132:133] op_sel_hi:[1,0]\n"
"v_pk_mul_f32 v[190:191], v[190:191], v[132:133] op_sel_hi:[1,0]\n"
"v_cvt_pk_bf16_f32 v176, v176, v177\n"
"buffer_store_dwordx4 v[196:199], v148, s[48:51], s45 offen offset:3328 nt\n"
"v_cvt_pk_bf16_f32 v177, v178, v179\n"
"v_cvt_pk_bf16_f32 v180, v180, v181\n"
"v_cvt_pk_bf16_f32 v181, v182, v183\n"
"v_cvt_pk_bf16_f32 v184, v184, v185\n"
"v_cvt_pk_bf16_f32 v185, v186, v187\n"
"v_cvt_pk_bf16_f32 v188, v188, v189\n"
"v_cvt_pk_bf16_f32 v189, v190, v191\n"
"s_waitcnt lgkmcnt(12)\n"
"v_add_f32_e32 v253, v253, v151\n"
"ds_write_b64 v142, v[176:177] offset:128\n"
"v_mov_b32_e32 v152, v253\n"
"s_nop 1\n"
"v_permlane32_swap_b32 v253, v152\n"
"v_add_f32_e32 v253, v253, v152\n"
"ds_write_b64 v143, v[180:181] offset:128\n"
"v_mov_b32_e32 v152, v253\n"
"s_waitcnt lgkmcnt(3)\n"
"buffer_store_dwordx4 v[200:203], v147, s[48:51], s43 offen offset:0 nt\n"
"v_permlane16_swap_b32 v253, v152\n"
"v_add_f32_e32 v253, v253, v152\n"
"ds_write_b64 v144, v[184:185] offset:128\n"
"v_cmp_nle_f32 vcc, s40, v253\n"
"v_rcp_f32_e32 v140, v253\n"
"s_nop 0\n"
"s_or_b64 s[74:75], s[74:75], vcc\n"
"v_cmp_nge_f32 vcc, s41, v253\n"
"ds_write_b64 v145, v[188:189] offset:128\n"
"v_pk_add_f32 v[80:81], v[80:81], v[216:217]\n"
"v_pk_add_f32 v[82:83], v[82:83], v[218:219]\n"
"v_pk_add_f32 v[88:89], v[88:89], v[220:221]\n"
"ds_read_b128 v[208:211], v146 offset:128\n"
"v_pk_add_f32 v[90:91], v[90:91], v[222:223]\n"
"v_pk_add_f32 v[96:97], v[96:97], v[224:225]\n"
"v_pk_add_f32 v[98:99], v[98:99], v[226:227]\n"
"s_waitcnt lgkmcnt(5)\n"
"buffer_store_dwordx4 v[204:207], v148, s[48:51], s43 offen offset:3072 nt\n"
"v_pk_add_f32 v[104:105], v[104:105], v[228:229]\n"
"ds_read_b128 v[212:215], v146 offset:3200\n"
"v_pk_add_f32 v[106:107], v[106:107], v[230:231]\n"
"v_pk_add_f32 v[112:113], v[112:113], v[232:233]\n"
"v_pk_add_f32 v[114:115], v[114:115], v[234:235]\n"
"v_pk_add_f32 v[120:121], v[120:121], v[236:237]\n"
"v_pk_add_f32 v[122:123], v[122:123], v[238:239]\n"
"s_or_b64 s[74:75], s[74:75], vcc\n"
"v_pk_mul_f32 v[80:81], v[80:81], v[140:141] op_sel_hi:[1,0]\n"
"v_pk_mul_f32 v[82:83], v[82:83], v[140:141] op_sel_hi:[1,0]\n"
"v_pk_mul_f32 v[88:89], v[88:89], v[140:141] op_sel_hi:[1,0]\n"
"v_pk_mul_f32 v[90:91], v[90:91], v[140:141] op_sel_hi:[1,0]\n"
"v_pk_mul_f32 v[96:97], v[96:97], v[140:141] op_sel_hi:[1,0]\n"
"v_pk_mul_f32 v[98:99], v[98:99], v[140:141] op_sel_hi:[1,0]\n"
"v_pk_mul_f32 v[104:105], v[104:105], v[140:141] op_sel_hi:[1,0]\n"
"v_pk_mul_f32 v[106:107], v[106:107], v[140:141] op_sel_hi:[1,0]\n"
"v_pk_mul_f32 v[112:113], v[112:113], v[140:141] op_sel_hi:[1,0]\n"
"v_pk_mul_f32 v[114:115], v[114:115], v[140:141] op_sel_hi:[1,0]\n"
"s_waitcnt lgkmcnt(1)\n"
"buffer_store_dwordx4 v[208:211], v147, s[48:51], s42 offen offset:128 nt\n"
"v_pk_mul_f32 v[120:121], v[120:121], v[140:141] op_sel_hi:[1,0]\n"
"v_pk_mul_f32 v[122:123], v[122:123], v[140:141] op_sel_hi:[1,0]\n"
"v_cvt_pk_bf16_f32 v80, v80, v81\n"
"v_cvt_pk_bf16_f32 v81, v82, v83\n"
"v_cvt_pk_bf16_f32 v88, v88, v89\n"
"v_cvt_pk_bf16_f32 v89, v90, v91\n"
"v_cvt_pk_bf16_f32 v96, v96, v97\n"
"v_cvt_pk_bf16_f32 v97, v98, v99\n"
"v_cvt_pk_bf16_f32 v104, v104, v105\n"
"v_cvt_pk_bf16_f32 v105, v106, v107\n"
"v_cvt_pk_bf16_f32 v112, v112, v113\n"
"v_cvt_pk_bf16_f32 v113, v114, v115\n"
"s_waitcnt lgkmcnt(0)\n"
"buffer_store_dwordx4 v[212:215], v148, s[48:51], s42 offen offset:3200 nt\n"
"v_cvt_pk_bf16_f32 v120, v120, v121\n"
"v_cvt_pk_bf16_f32 v121, v122, v123\n"
"buffer_store_dwordx2 v[80:81], v154, s[48:51], s69 offen offset:0\n"
"buffer_store_dwordx2 v[88:89], v154, s[48:51], s69 offen offset:64\n"
"buffer_store_dwordx2 v[96:97], v154, s[48:51], s69 offen offset:128\n"
"buffer_store_dwordx2 v[104:105], v154, s[48:51], s69 offen offset:192\n"
"buffer_store_dwordx2 v[112:113], v154, s[48:51], s69 offen offset:256\n"
"buffer_store_dwordx2 v[120:121], v154, s[48:51], s69 offen offset:320\n"
"s_cmp_lg_u64 s[74:75], 0\n"
"s_cselect_b32 s69, 1, 0\n"
"v_mov_b32_e32 v151, s69\n"
"s_lshl_b32 s70, s67, 2\n"
"s_add_u32 s70, s70, s72\n"
"s_add_u32 s70, s70, 160000\n"
"v_mov_b32_e32 v152, s70\n"
"ds_write_b32 v152, v151 offset:0\n"
"v_accvgpr_read_b32 v160, a156\n"
"v_accvgpr_read_b32 v161, a157\n"
"v_accvgpr_read_b32 v162, a158\n"
"v_accvgpr_read_b32 v163, a159\n"
"v_accvgpr_read_b32 v164, a164\n"
"v_accvgpr_read_b32 v165, a165\n"
"v_accvgpr_read_b32 v166, a166\n"
"v_accvgpr_read_b32 v167, a167\n"
"v_accvgpr_read_b32 v168, a172\n"
"v_accvgpr_read_b32 v169, a173\n"
"v_accvgpr_read_b32 v170, a174\n"
"v_accvgpr_read_b32 v171, a175\n"
"v_accvgpr_read_b32 v172, a180\n"
"v_accvgpr_read_b32 v173, a181\n"
"v_accvgpr_read_b32 v174, a182\n"
"v_accvgpr_read_b32 v175, a183\n"
"v_pk_mul_f32 v[160:161], v[160:161], v[134:135] op_sel_hi:[1,0]\n"
"v_pk_mul_f32 v[162:163], v[162:163], v[134:135] op_sel_hi:[1,0]\n"
"v_pk_mul_f32 v[164:165], v[164:165], v[134:135] op_sel_hi:[1,0]\n"
"v_pk_mul_f32 v[166:167], v[166:167], v[134:135] op_sel_hi:[1,0]\n"
"v_pk_mul_f32 v[168:169], v[168:169], v[134:135] op_sel_hi:[1,0]\n"
"v_pk_mul_f32 v[170:171], v[170:171], v[134:135] op_sel_hi:[1,0]\n"
"v_pk_mul_f32 v[172:173], v[172:173], v[134:135] op_sel_hi:[1,0]\n"
"v_pk_mul_f32 v[174:175], v[174:175], v[134:135] op_sel_hi:[1,0]\n"
"v_cvt_pk_bf16_f32 v160, v160, v161\n"
"v_cvt_pk_bf16_f32 v161, v162, v163\n"
"v_cvt_pk_bf16_f32 v164, v164, v165\n"
"v_cvt_pk_bf16_f32 v165, v166, v167\n"
"v_cvt_pk_bf16_f32 v168, v168, v169\n"
"v_cvt_pk_bf16_f32 v169, v170, v171\n"
"v_cvt_pk_bf16_f32 v172, v172, v173\n"
"v_cvt_pk_bf16_f32 v173, v174, v175\n"
"v_accvgpr_read_b32 v176, a184\n"
"ds_write_b64 v142, v[160:161] offset:6272\n"
"v_accvgpr_read_b32 v177, a185\n"
"v_accvgpr_read_b32 v178, a186\n"
"v_accvgpr_read_b32 v179, a187\n"
"ds_write_b64 v143, v[164:165] offset:6272\n"
"v_accvgpr_read_b32 v180, a192\n"
"v_accvgpr_read_b32 v181, a193\n"
"v_accvgpr_read_b32 v182, a194\n"
"ds_write_b64 v144, v[168:169] offset:6272\n"
"v_accvgpr_read_b32 v183, a195\n"
"v_accvgpr_read_b32 v184, a200\n"
"v_accvgpr_read_b32 v185, a201\n"
"ds_write_b64 v145, v[172:173] offset:6272\n"
"v_accvgpr_read_b32 v186, a202\n"
"v_accvgpr_read_b32 v187, a203\n"
"v_accvgpr_read_b32 v188, a208\n"
"ds_read_b128 v[192:195], v146 offset:6272\n"
"v_accvgpr_read_b32 v189, a209\n"
"v_accvgpr_read_b32 v190, a210\n"
"v_accvgpr_read_b32 v191, a211\n"
"ds_read_b128 v[196:199], v146 offset:9344\n"
"v_pk_mul_f32 v[176:177], v[176:177], v[132:133] op_sel_hi:[1,0]\n"
"v_pk_mul_f32 v[178:179], v[178:179], v[132:133] op_sel_hi:[1,0]\n"
"v_pk_mul_f32 v[180:181], v[180:181], v[132:133] op_sel_hi:[1,0]\n"
"v_pk_mul_f32 v[182:183], v[182:183], v[132:133] op_sel_hi:[1,0]\n"
"v_pk_mul_f32 v[184:185], v[184:185], v[132:133] op_sel_hi:[1,0]\n"
"v_pk_mul_f32 v[186:187], v[186:187], v[132:133] op_sel_hi:[1,0]\n"
"v_pk_mul_f32 v[188:189], v[188:189], v[132:133] op_sel_hi:[1,0]\n"
"v_pk_mul_f32 v[190:191], v[190:191], v[132:133] op_sel_hi:[1,0]\n"
"v_cvt_pk_bf16_f32 v176, v176, v177\n"
"v_cvt_pk_bf16_f32 v177, v178, v179\n"
"v_cvt_pk_bf16_f32 v180, v180, v181\n"
"v_cvt_pk_bf16_f32 v181, v182, v183\n"
"v_cvt_pk_bf16_f32 v184, v184, v185\n"
"v_cvt_pk_bf16_f32 v185, v186, v187\n"
"v_cvt_pk_bf16_f32 v188, v188, v189\n"
"v_cvt_pk_bf16_f32 v189, v190, v191\n"
"v_accvgpr_read_b32 v160, a188\n"
"s_waitcnt lgkmcnt(1)\n"
"buffer_store_dwordx4 v[192:195], v147, s[48:51], s43 offen offset:128 nt\n"
"v_accvgpr_read_b32 v161, a189\n"
"ds_write_b64 v142, v[176:177] offset:256\n"
"v_accvgpr_read_b32 v162, a190\n"
"v_accvgpr_read_b32 v163, a191\n"
"v_accvgpr_read_b32 v164, a196\n"
"ds_write_b64 v143, v[180:181] offset:256\n"
"v_accvgpr_read_b32 v165, a197\n"
"v_accvgpr_read_b32 v166, a198\n"
"v_accvgpr_read_b32 v167, a199\n"
"ds_write_b64 v144, v[184:185] offset:256\n"
"v_accvgpr_read_b32 v168, a204\n"
"v_accvgpr_read_b32 v169, a205\n"
"v_accvgpr_read_b32 v170, a206\n"
"ds_write_b64 v145, v[188:189] offset:256\n"
"v_accvgpr_read_b32 v171, a207\n"
"v_accvgpr_read_b32 v172, a212\n"
"s_waitcnt lgkmcnt(4)\n"
"buffer_store_dwordx4 v[196:199], v148, s[48:51], s43 offen offset:3200 nt\n"
"v_accvgpr_read_b32 v173, a213\n"
"ds_read_b128 v[200:203], v146 offset:256\n"
"v_accvgpr_read_b32 v174, a214\n"
"v_accvgpr_read_b32 v175, a215\n"
"v_pk_mul_f32 v[160:161], v[160:161], v[134:135] op_sel_hi:[1,0]\n"
"ds_read_b128 v[204:207], v146 offset:3328\n"
"v_pk_mul_f32 v[162:163], v[162:163], v[134:135] op_sel_hi:[1,0]\n"
"v_pk_mul_f32 v[164:165], v[164:165], v[134:135] op_sel_hi:[1,0]\n"
"v_pk_mul_f32 v[166:167], v[166:167], v[134:135] op_sel_hi:[1,0]\n"
"v_pk_mul_f32 v[168:169], v[168:169], v[134:135] op_sel_hi:[1,0]\n"
"v_pk_mul_f32 v[170:171], v[170:171], v[134:135] op_sel_hi:[1,0]\n"
"v_pk_mul_f32 v[172:173], v[172:173], v[134:135] op_sel_hi:[1,0]\n"
"v_pk_mul_f32 v[174:175], v[174:175], v[134:135] op_sel_hi:[1,0]\n"
"v_cvt_pk_bf16_f32 v160, v160, v161\n"
"v_cvt_pk_bf16_f32 v161, v162, v163\n"
"v_cvt_pk_bf16_f32 v164, v164, v165\n"
"v_cvt_pk_bf16_f32 v165, v166, v167\n"
"v_cvt_pk_bf16_f32 v168, v168, v169\n"
"v_cvt_pk_bf16_f32 v169, v170, v171\n"
"v_cvt_pk_bf16_f32 v172, v172, v173\n"
"v_cvt_pk_bf16_f32 v173, v174, v175\n"
"ds_write_b64 v142, v[160:161] offset:6400\n"
"ds_write_b64 v143, v[164:165] offset:6400\n"
"ds_write_b64 v144, v[168:169] offset:6400\n"
"ds_write_b64 v145, v[172:173] offset:6400\n"
"ds_read_b128 v[208:211], v146 offset:6400\n"
"ds_read_b128 v[212:215], v146 offset:9472\n"
"s_waitcnt lgkmcnt(7)\n"
"buffer_store_dwordx4 v[200:203], v147, s[48:51], s42 offen offset:256 nt\n"
"s_waitcnt lgkmcnt(6)\n"
"buffer_store_dwordx4 v[204:207], v148, s[48:51], s42 offen offset:3328 nt\n"
"s_waitcnt lgkmcnt(1)\n"
"buffer_store_dwordx4 v[208:211], v147, s[48:51], s43 offen offset:256 nt\n"
"s_waitcnt lgkmcnt(0)\n"
"buffer_store_dwordx4 v[212:215], v148, s[48:51], s43 offen offset:3328 nt\n"
"s_waitcnt lgkmcnt(0)\n"
"s_barrier\n"
"s_add_u32 s70, s72, 160000\n"
"v_mov_b32_e32 v152, s70\n"
"ds_read_b128 v[200:203], v152 offset:0\n"
"s_waitcnt lgkmcnt(0)\n"
"v_or_b32_e32 v200, v200, v201\n"
"v_or_b32_e32 v202, v202, v203\n"
"v_or_b32_e32 v200, v200, v202\n"
"s_nop 0\n"
"v_readfirstlane_b32 %0, v200\n"
      : "=s"(unsafe)
      : "s"(__builtin_amdgcn_kernarg_segment_ptr()), "s"(head), "s"(wid), "s"(lds0)
      : "memory", "vcc", "scc", "v0","v1","v2","v3","v4","v5","v6","v7","v8","v9","v10","v11","v12","v13","v14","v15","v16","v17","v18","v19","v20","v21","v22","v23","v24","v25","v26","v27","v28","v29","v30","v31","v32","v33","v34","v35","v36","v37","v38","v39","v40","v41","v42","v43","v44","v45","v46","v47","v48","v49","v50","v51","v52","v53","v54","v55","v56","v57","v58","v59","v60","v61","v62","v63","v64","v65","v66","v67","v68","v69","v70","v71","v72","v73","v74","v75","v76","v77","v78","v79","v80","v81","v82","v83","v84","v85","v86","v87","v88","v89","v90","v91","v92","v93","v94","v95","v96","v97","v98","v99","v100","v101","v102","v103","v104","v105","v106","v107","v108","v109","v110","v111","v112","v113","v114","v115","v116","v117","v118","v119","v120","v121","v122","v123","v124","v125","v126","v127","v128","v129","v130","v131","v132","v133","v134","v135","v136","v137","v138","v139","v140","v141","v142","v143","v144","v145","v146","v147","v148","v149","v150","v151","v152","v153","v154","v155","v156","v157","v158","v159","v160","v161","v162","v163","v164","v165","v166","v167","v168","v169","v170","v171","v172","v173","v174","v175","v176","v177","v178","v179","v180","v181","v182","v183","v184","v185","v186","v187","v188","v189","v190","v191","v192","v193","v194","v195","v196","v197","v198","v199","v200","v201","v202","v203","v204","v205","v206","v207","v208","v209","v210","v211","v212","v213","v214","v215","v216","v217","v218","v219","v220","v221","v222","v223","v224","v225","v226","v227","v228","v229","v230","v231","v232","v233","v234","v235","v236","v237","v238","v239","v240","v241","v242","v243","v244","v245","v246","v247","v248","v249","v250","v251","v252","v253","v254","v255", "a0","a1","a2","a3","a4","a5","a6","a7","a8","a9","a10","a11","a12","a13","a14","a15","a16","a17","a18","a19","a20","a21","a22","a23","a24","a25","a26","a27","a28","a29","a30","a31","a32","a33","a34","a35","a36","a37","a38","a39","a40","a41","a42","a43","a44","a45","a46","a47","a48","a49","a50","a51","a52","a53","a54","a55","a56","a57","a58","a59","a60","a61","a62","a63","a64","a65","a66","a67","a68","a69","a70","a71","a72","a73","a74","a75","a76","a77","a78","a79","a80","a81","a82","a83","a84","a85","a86","a87","a88","a89","a90","a91","a92","a93","a94","a95","a96","a97","a98","a99","a100","a101","a102","a103","a104","a105","a106","a107","a108","a109","a110","a111","a112","a113","a114","a115","a116","a117","a118","a119","a120","a121","a122","a123","a124","a125","a126","a127","a128","a129","a130","a131","a132","a133","a134","a135","a136","a137","a138","a139","a140","a141","a142","a143","a144","a145","a146","a147","a148","a149","a150","a151","a152","a153","a154","a155","a156","a157","a158","a159","a160","a161","a162","a163","a164","a165","a166","a167","a168","a169","a170","a171","a172","a173","a174","a175","a176","a177","a178","a179","a180","a181","a182","a183","a184","a185","a186","a187","a188","a189","a190","a191","a192","a193","a194","a195","a196","a197","a198","a199","a200","a201","a202","a203","a204","a205","a206","a207","a208","a209","a210","a211","a212","a213","a214","a215","a216","a217","a218","a219","a220","a221","a222","a223","a224","a225","a226","a227","a228","a229","a230","a231","a232","a233","a234","a235","a236","a237","a238","a239","a240","a241","a242","a243","a244","a245","a246","a247","a248","a249","a250","a251","a252","a253","a254","a255", "s38","s39","s40","s41","s42","s43","s44","s45","s46","s47","s48","s49","s50","s51","s52","s53","s54","s55","s56","s57","s58","s59","s60","s61","s62","s63","s64","s65","s66","s67","s68","s69","s70","s71","s72","s73","s74","s75","s76","s77","s78","s79","s80","s81","s82","s83","s84","s85","s86","s87","s88","s89","s90","s91","s92","s93","s94","s95","s96","s97","s98","s99");
  if (unsafe) {
    asm volatile("s_waitcnt vmcnt(0) lgkmcnt(0)\n\ts_barrier" ::: "memory");
    slow_path(q, k, v, o, head, wid, lds);
  }
}

extern "C" void kernel_launch(void* const* d_in, const int* in_sizes, int n_in, void* d_out, int out_size, void* d_ws, size_t ws_size, hipStream_t stream) {
  (void)hipFuncSetAttribute((const void*)attn_fwd, hipFuncAttributeMaxDynamicSharedMemorySize, LDSB);
  attn_fwd<<<256, 256, LDSB, stream>>>((const unsigned short*)d_in[0], (const unsigned short*)d_in[1], (const unsigned short*)d_in[2], (unsigned short*)d_out
  );
}
```
